```python
import torch

def _softmax(x, axis=-1):
    return torch.softmax(x, dim=axis)
def _gelu(x):
    return torch.nn.functional.gelu(x, approximate="tanh")
def _silu(x):
    return torch.nn.functional.silu(x)
def _sigmoid(x):
    return torch.sigmoid(x)
def _layernorm(x, axis=-1, eps=1e-5):
    m = x.mean(dim=axis, keepdim=True)
    v = x.var(dim=axis, keepdim=True, unbiased=False)
    return (x - m) / torch.sqrt(v + eps)
def _scatter_add(tgt, idx, src, axis=0):
    return tgt.clone().index_add_(axis, idx, src)
def _segment_sum(src, seg, n):
    out = torch.zeros((n,) + src.shape[1:], dtype=src.dtype)
    return out.index_add_(0, seg, src)
def _pad(x, pw):
    flat = tuple(v for pair in reversed(pw) for v in pair)
    return torch.nn.functional.pad(x, flat)

kernel_name = 'gather_scatter__edge_featured_mpnn_gatedgcn__graph'

def setup_inputs():

    _w = (lambda a: (lambda t: t.to(torch.bfloat16) if t.is_floating_point() else t)(torch.as_tensor(a)))
    N, deg, D, De = 12500, 32, 128, max(4, 1)
    E = N * deg
    return {
        "x": _w((lambda *s: torch.randn(*s, dtype=torch.bfloat16) * 0.4)(N, D)),
        "e_attr": _w((lambda *s: torch.randn(*s, dtype=torch.bfloat16) * 0.8)(E, De)),
        "src": _w((lambda lo, hi, *s: torch.randint(lo, hi, s))(0, N, E).to(torch.int64)),
        "dst": _w((lambda lo, hi, *s: torch.randint(lo, hi, s))(0, N, E).to(torch.int64)),
        "w_a": _w((lambda *s: torch.randn(*s, dtype=torch.bfloat16) * 0.4)(D, D)),
        "w_b": _w((lambda *s: torch.randn(*s, dtype=torch.bfloat16) * 0.4)(D, D)),
        "w_c": _w((lambda *s: torch.randn(*s, dtype=torch.bfloat16) * 0.8)(De, D)),
        "w_u": _w((lambda *s: torch.randn(*s, dtype=torch.bfloat16) * 0.05)(D, D)),
        "w_v": _w((lambda *s: torch.randn(*s, dtype=torch.bfloat16) * 0.05)(D, D)),
    }

def reference(x, e_attr, src, dst, w_a, w_b, w_c, w_u, w_v):
    N, D = x.shape
    hv = x @ w_v
    g = x[dst] @ w_a + x[src] @ w_b + e_attr @ w_c
    eta = _sigmoid(g)
    denom = _segment_sum(eta, dst, N) + 1e-6
    eta_n = eta / denom[dst]
    agg = _segment_sum(eta_n * hv[src], dst, N)
    z = x @ w_u + agg
    relu_z = torch.where(z > 0, z, 0 * z)
    return x + relu_z
```

```cpp
#include <hip/hip_runtime.h>
#include <hip/hip_bf16.h>

static __device__ __forceinline__ float bf2f(unsigned short u) { return __uint_as_float(((unsigned)u) << 16); }

__global__ void k_gemm(const unsigned short* x, const unsigned short* wa, const unsigned short* wb,
                       const unsigned short* wu, const unsigned short* wv, float* A, float* B, float* U, float* V, int N) {
  int n = blockIdx.x; int d = threadIdx.x;
  __shared__ float xs[128];
  xs[d] = bf2f(x[n * 128 + d]);
  __syncthreads();
  float a = 0, b = 0, u = 0, v = 0;
  for (int k = 0; k < 128; ++k) {
    float xv = xs[k];
    a += xv * bf2f(wa[k * 128 + d]); b += xv * bf2f(wb[k * 128 + d]);
    u += xv * bf2f(wu[k * 128 + d]); v += xv * bf2f(wv[k * 128 + d]);
  }
  A[n * 128 + d] = a; B[n * 128 + d] = b; U[n * 128 + d] = u; V[n * 128 + d] = v;
}
__global__ void k_edge(const unsigned short* ea, const int* src, const int* dst, const unsigned short* wc,
                       const float* A, const float* B, const float* V, float* num, float* den, int E) {
  int e = blockIdx.x * 2 + (threadIdx.x >> 7); int d = threadIdx.x & 127;
  if (e >= E) return;
  int s = src[e], t = dst[e];
  float g = A[t * 128 + d] + B[s * 128 + d];
  for (int k = 0; k < 4; ++k) g += bf2f(ea[e * 4 + k]) * bf2f(wc[k * 128 + d]);
  float eta = 1.f / (1.f + __expf(-g));
  atomicAdd(&den[t * 128 + d], eta);
  atomicAdd(&num[t * 128 + d], eta * V[s * 128 + d]);
}
__global__ void k_fin(const unsigned short* x, const float* U, const float* num, const float* den, __hip_bfloat16* out, int n) {
  int i = blockIdx.x * 256 + threadIdx.x; if (i >= n) return;
  float z = U[i] + num[i] / (den[i] + 1e-6f);
  out[i] = __float2bfloat16(bf2f(x[i]) + (z > 0 ? z : 0.f));
}
extern "C" void kernel_launch(void* const* d_in, const int* in_sizes, int n_in, void* d_out, int out_size, void* d_ws, size_t ws_size, hipStream_t stream) {
  int N = in_sizes[0] / 128, E = in_sizes[2];
  float* ws = (float*)d_ws; size_t sz = (size_t)N * 128;
  float *A = ws, *B = ws + sz, *U = ws + 2 * sz, *V = ws + 3 * sz, *num = ws + 4 * sz, *den = ws + 5 * sz;
  hipMemsetAsync(num, 0, 2 * sz * 4, stream);
  k_gemm<<<N, 128, 0, stream>>>((const unsigned short*)d_in[0], (const unsigned short*)d_in[4], (const unsigned short*)d_in[5], (const unsigned short*)d_in[7], (const unsigned short*)d_in[8], A, B, U, V, N);
  k_edge<<<(E + 1) / 2, 256, 0, stream>>>((const unsigned short*)d_in[1], (const int*)d_in[2], (const int*)d_in[3], (const unsigned short*)d_in[6], A, B, V, num, den, E);
  k_fin<<<(N * 128 + 255) / 256, 256, 0, stream>>>((const unsigned short*)d_in[0], U, num, den, (__hip_bfloat16*)d_out, N * 128);
}
```

```cpp
#include <hip/hip_runtime.h>
#include <stdint.h>

typedef unsigned int u32;
typedef unsigned short u16;
typedef unsigned long long u64;
using s16x4 = __attribute__((ext_vector_type(4))) short;
using s16x8 = __attribute__((ext_vector_type(8))) short;
using h16x4 = __attribute__((ext_vector_type(4))) _Float16;
using h16x2 = __attribute__((ext_vector_type(2))) _Float16;
using b16x2 = __attribute__((ext_vector_type(2))) __bf16;
using f32x4 = __attribute__((ext_vector_type(4))) float;
using f32x2 = __attribute__((ext_vector_type(2))) float;
using u32x2 = __attribute__((ext_vector_type(2))) u32;
using u32x3 = __attribute__((ext_vector_type(3))) u32;
using u32x4 = __attribute__((ext_vector_type(4))) u32;
#define LAS __attribute__((address_space(3)))
#define DI __device__ __forceinline__

constexpr int NCH = 128;
constexpr int EPT = 8;
constexpr int K1T = 512;
constexpr int GBLK = 7;
constexpr float NLOG2E = -1.4426950408889634f;
constexpr int WIN = 384;

typedef LAS char* lptr;
template <class T> DI T lds_ld(lptr a) { return *(LAS T*)a; }
template <class T> DI void lds_st(lptr a, T v) { *(LAS T*)a = v; }
DI s16x4 lds_tr(lptr a) { return __builtin_amdgcn_ds_read_tr16_b64_v4i16((LAS s16x4*)a); }
DI u32 pk_bf16(float lo, float hi) { f32x2 v = {lo, hi}; return __builtin_bit_cast(u32, __builtin_convertvector(v, b16x2)); }
DI float bf_lo(u32 w) { return __uint_as_float(w << 16); }
DI float bf_hi(u32 w) { return __uint_as_float(w & 0xffff0000u); }

DI void dma_g(u32 m0v, const void* base, u32 voff) {
  asm volatile("s_mov_b32 m0, %0\n\ts_nop 0\n\tglobal_load_lds_dwordx4 %1, %2" ::"s"(m0v), "v"(voff), "s"(base) : "memory");
}
DI void dma_b(u32 m0v, u32x4 rsrc, u32x2 io) {
  asm volatile("s_mov_b32 m0, %0\n\ts_nop 0\n\tbuffer_load_dwordx4 %1, %2, 0 idxen offen lds" ::"s"(m0v), "v"(io), "s"(rsrc) : "memory");
}

template <int CTRL, int RM> DI u32 dpp0(u32 v) { return (u32)__builtin_amdgcn_update_dpp(0, (int)v, CTRL, RM, 0xf, false); }
DI u32 wave_scan_add(u32 v) {
  v += dpp0<0x111, 0xf>(v); v += dpp0<0x112, 0xf>(v); v += dpp0<0x114, 0xf>(v); v += dpp0<0x118, 0xf>(v);
  v += dpp0<0x142, 0xa>(v); v += dpp0<0x143, 0xc>(v);
  return v;
}
DI u32 umax(u32 a, u32 b) { return a > b ? a : b; }
DI u32 wave_scan_max(u32 v) {
  v = umax(v, dpp0<0x111, 0xf>(v)); v = umax(v, dpp0<0x112, 0xf>(v)); v = umax(v, dpp0<0x114, 0xf>(v)); v = umax(v, dpp0<0x118, 0xf>(v));
  v = umax(v, dpp0<0x142, 0xa>(v)); v = umax(v, dpp0<0x143, 0xc>(v));
  return v;
}
DI u32 divm(u32 x, u64 magic) { return (u32)(((u64)x * magic) >> 48); }

#define STAMP(k)
#define STAMP_DECL
#define STAMP_OUT(id)
__global__ __launch_bounds__(K1T) void k_prep(const u16* __restrict__ x, const u16* __restrict__ ea, const int* __restrict__ src,
                                             const int* __restrict__ dst, const u16* __restrict__ wb, const u16* __restrict__ wc,
                                             const u16* __restrict__ wv, char* __restrict__ T, u32* __restrict__ rec,
                                             u32* __restrict__ off, u32* __restrict__ vmax, u16* __restrict__ wcp, u64 magN,
                                             u64 magB, int N, int E, int Bk, int CH, int RPG) {
  extern __shared__ __attribute__((aligned(16))) char smem[];
  const lptr L = (lptr)smem;
  const u32 lb = (u32)(uintptr_t)L;
  const int tid = threadIdx.x, lane = tid & 63;
  const int w = __builtin_amdgcn_readfirstlane(tid >> 6);
  STAMP_DECL; STAMP(0);
  if ((int)blockIdx.x < NCH) {
    const int c = blockIdx.x;
    const int e0 = c * CH, e1 = min(E, e0 + CH);
    u32* hist = (u32*)smem;
    u32* wsum = hist + Bk;
    int dv[EPT], sv[EPT];
    u32x2 ev[EPT];
#pragma unroll
    for (int i = 0; i < EPT; ++i) {
      const int e = e0 + tid + i * K1T;
      const int ec = e < e1 ? e : e0;
      const int ee = ec < E ? ec : 0;
      dv[i] = dst[ee]; sv[i] = src[ee]; ev[i] = *(const u32x2*)(ea + (size_t)ee * 4);
    }
    for (int i = tid; i < Bk; i += K1T) hist[i] = 0;
    __syncthreads();
    STAMP(1);
    u32 bk[EPT], rk[EPT];
#pragma unroll
    for (int i = 0; i < EPT; ++i) {
      const int e = e0 + tid + i * K1T;
      bk[i] = divm((u32)(dv[i] + 1) * (u32)Bk - 1u, magN);
      rk[i] = 0;
      if (e < e1) rk[i] = atomicAdd(&hist[bk[i]], 1u);
    }
    __syncthreads();
    STAMP(2);
    const int per = (Bk + K1T - 1) / K1T;
    u32 loc = 0;
    for (int i = 0; i < per; ++i) { const int b = tid * per + i; loc += b < Bk ? hist[b] : 0u; }
    const u32 inc = wave_scan_add(loc);
    if (lane == 63) wsum[w] = inc;
    __syncthreads();
    u32 base = inc - loc;
    for (int i = 0; i < w; ++i) base += wsum[i];
    for (int i = 0; i < per; ++i) {
      const int b = tid * per + i;
      if (b < Bk) { const u32 cnt = hist[b]; hist[b] = base; off[(size_t)c * Bk + b] = base | (cnt << 16); base += cnt; }
    }
    __syncthreads();
    STAMP(3);
#pragma unroll
    for (int i = 0; i < EPT; ++i) {
      const int e = e0 + tid + i * K1T;
      if (e < e1) {
        const u32 pos = hist[bk[i]] + rk[i];
        const u32 dl = (u32)dv[i] - divm(bk[i] * (u32)N, magB);
        u32x3 r = {ev[i][0], ev[i][1], (u32)sv[i] | (dl << 28)};
        *(u32x3*)(rec + ((size_t)c * CH + pos) * 3) = r;
      }
    }
    STAMP(4);
    asm volatile("s_waitcnt vmcnt(0)" ::: "memory");
    STAMP(5);
    STAMP_OUT(4096 + blockIdx.x * 8 + w);
    return;
  }
  const int g = blockIdx.x - NCH;
  const int r0 = g * RPG;
  const int nrows = min(RPG, N - r0);
  const u32 XO = 65536, MO = XO + GBLK * 16 * 256;
  {
    const int rr = lane >> 4, pos = lane & 15;
#pragma unroll
    for (int i = 0; i < 8; ++i) {
      const int m = w * 8 + i;
      const int k = (m & 31) * 4 + rr;
      const int fw = (k & 1) | ((k & 2) << 2);
      dma_g(lb + m * 1024, (m >> 5) ? (const void*)wv : (const void*)wb, (u32)(k * 256 + ((pos ^ fw) << 4)));
    }
    for (int m = w; m < GBLK * 4; m += 8) {
      const int row = 4 * m + rr;
      const int gr = min(r0 + row, N - 1);
      dma_g(lb + XO + m * 1024, x, (u32)(gr * 256 + ((pos ^ (row & 15)) << 4)));
    }
    if (tid == 0) lds_st<u32>(L + MO, 0u);
    if (g == 0) {
      const int s = tid >> 8, cb = (tid >> 6) & 3, n = (tid >> 2) & 15, j = tid & 3;
      const int lam = 16 * (n >> 2) + 4 * cb + (n & 3);
      const float v = __uint_as_float((u32)wc[j * 128 + 64 * s + lam] << 16) * NLOG2E;
      wcp[tid] = (u16)(pk_bf16(v, 0.f) & 0xffffu);
    }
  }
  STAMP(1);
  asm volatile("s_waitcnt vmcnt(0)" ::: "memory");
  __syncthreads();
  STAMP(2);
  const int s = w >> 2, mat = (w >> 1) & 1, kq = w & 1;
  const int q = lane >> 4, qq = (lane >> 2) & 3, p = lane & 3;
  s16x8 wf[2][4];
#pragma unroll
  for (int be = 0; be < 2; ++be) {
    const int lam0 = mat ? (16 * (2 * (p & 1) + be) + 4 * (2 * kq + (p >> 1))) : (16 * p + 8 * kq + 4 * be);
    const int byte = 2 * (64 * s + lam0);
#pragma unroll
    for (int ks = 0; ks < 4; ++ks) {
      s16x4 h[2];
#pragma unroll
      for (int hh = 0; hh < 2; ++hh) {
        const int k = 32 * ks + 8 * q + 4 * hh + qq;
        const int fw = (k & 1) | ((k & 2) << 2);
        h[hh] = lds_tr(L + mat * 32768 + k * 256 + (((byte >> 4) ^ fw) << 4) + (byte & 8));
      }
      wf[be][ks] = __builtin_shufflevector(h[0], h[1], 0, 1, 2, 3, 4, 5, 6, 7);
    }
  }
  STAMP(3);
  float vm = 0.f;
  const int nl = lane & 15;
  for (int blk = 0; blk * 16 < nrows; ++blk) {
    f32x4 a0 = {0.f, 0.f, 0.f, 0.f}, a1 = {0.f, 0.f, 0.f, 0.f};
#pragma unroll
    for (int ks = 0; ks < 4; ++ks) {
      const s16x8 xf = lds_ld<s16x8>(L + XO + (blk * 16 + nl) * 256 + (((4 * ks + q) ^ nl) << 4));
      a0 = __builtin_amdgcn_mfma_f32_16x16x32_bf16(wf[0][ks], xf, a0, 0, 0, 0);
      a1 = __builtin_amdgcn_mfma_f32_16x16x32_bf16(wf[1][ks], xf, a1, 0, 0, 0);
    }
    u32x4 o;
    const bool valid = blk * 16 + nl < nrows;
    if (mat == 0) {
      o[0] = pk_bf16(a0[0] * NLOG2E, a0[1] * NLOG2E); o[1] = pk_bf16(a0[2] * NLOG2E, a0[3] * NLOG2E);
      o[2] = pk_bf16(a1[0] * NLOG2E, a1[1] * NLOG2E); o[3] = pk_bf16(a1[2] * NLOG2E, a1[3] * NLOG2E);
    } else {
      h16x2 h0 = {(_Float16)a0[0], (_Float16)a0[1]}, h1 = {(_Float16)a0[2], (_Float16)a0[3]};
      h16x2 h2 = {(_Float16)a1[0], (_Float16)a1[1]}, h3 = {(_Float16)a1[2], (_Float16)a1[3]};
      o[0] = __builtin_bit_cast(u32, h0); o[1] = __builtin_bit_cast(u32, h1);
      o[2] = __builtin_bit_cast(u32, h2); o[3] = __builtin_bit_cast(u32, h3);
      u32 mx = 0;
#pragma unroll
      for (int i = 0; i < 4; ++i) { mx = umax(mx, __float_as_uint(a0[i]) & 0x7fffffffu); mx = umax(mx, __float_as_uint(a1[i]) & 0x7fffffffu); }
      if (valid) vm = __uint_as_float(umax(__float_as_uint(vm), mx));
    }
    if (valid) *(u32x4*)(T + ((size_t)s * N + r0 + blk * 16 + nl) * 256 + mat * 128 + (4 * kq + q) * 16) = o;
  }
  STAMP(4);
  asm volatile("s_waitcnt vmcnt(0)" ::: "memory");
  STAMP(5);
  STAMP_OUT(4096 + blockIdx.x * 8 + w);
  if (mat == 1) {
    const u32 m = wave_scan_max(__float_as_uint(vm));
    if (lane == 63) atomicMax((u32*)(smem + MO), m);
  }
  __syncthreads();
  if (tid == 0) vmax[g] = lds_ld<u32>(L + MO);
}

constexpr u32 STG = 8192;
constexpr u32 OHR = 4096, IDR = 512;
constexpr u32 K2LDS = 4 * STG + 4 * (OHR + IDR);

__global__ __launch_bounds__(256, 3) void k_edge(const u16* __restrict__ x, const u16* __restrict__ wa, const u16* __restrict__ wu,
                                                const u16* __restrict__ wcp, const char* __restrict__ T,
                                                const u32* __restrict__ rec, const u32* __restrict__ off,
                                                const u32* __restrict__ vmax, u16* __restrict__ out, u64 magB, int N, int Bk,
                                                int CH, int nvm) {
  extern __shared__ __attribute__((aligned(16))) char smem[];
  const lptr L = (lptr)smem;
  const u32 lb = (u32)(uintptr_t)L;
  const int tid = threadIdx.x, lane = tid & 63;
  const int w = __builtin_amdgcn_readfirstlane(tid >> 6);
  STAMP_DECL; STAMP(0);
  const int r8 = blockIdx.x & 7, slice = r8 & 1;
  const int b = ((blockIdx.x >> 3) * 4 + (r8 >> 1)) * 4 + w;
  const int n0 = (int)divm((u32)b * (u32)N, magB), n1 = (int)divm((u32)(b + 1) * (u32)N, magB);
  const int nb = n1 - n0;
  const int q = lane >> 4, nl = lane & 15, qq = (lane >> 2) & 3, p = lane & 3;
  const lptr stg = L + w * STG, ohr = L + 4 * STG + w * (OHR + IDR), idr = ohr + OHR;
  const u32 stgm0 = lb + w * STG;

  {
    const int rr = lane >> 3, pos = lane & 7;
#pragma unroll
    for (int i = 0; i < 8; ++i) {
      const int m = w * 8 + i;
      const int k = (m & 15) * 8 + rr;
      dma_g(lb + m * 1024, (m >> 4) ? (const void*)wu : (const void*)wa, (u32)(k * 256 + slice * 128 + ((pos ^ ((k >> 1) & 1)) << 4)));
    }
  }
  u32 o0 = off[(size_t)lane * Bk + b], o1 = off[(size_t)(lane + 64) * Bk + b];
  const int xrow = min(n0 + min(nl, max(nb - 1, 0)), N - 1);
  s16x8 xf[4];
#pragma unroll
  for (int ks = 0; ks < 4; ++ks) xf[ks] = *(const s16x8*)(x + (size_t)xrow * 128 + ks * 32 + q * 8);
  u32x2 wcv[4];
#pragma unroll
  for (int cb = 0; cb < 4; ++cb) wcv[cb] = *(const u32x2*)(wcp + ((slice * 4 + cb) * 16 + nl) * 4);
  u32 vmx = 0;
  for (int i = lane; i < nvm; i += 64) vmx = umax(vmx, vmax[i]);
  vmx = __builtin_amdgcn_readlane(wave_scan_max(vmx), 63);
  int ea_ = 15 - ((int)(vmx >> 23) - 126);
  ea_ = ea_ > 15 ? 15 : ea_;
  const float cneg = (float)(-ea_), cadd = __uint_as_float((u32)(127 - ea_) << 23);
  const float epsv = 1e-6f * 1.875f * __uint_as_float((u32)(127 + ea_) << 23);

  const int ntot = (int)__builtin_amdgcn_readlane(wave_scan_add((o0 >> 16) + (o1 >> 16)), 63);

  const u32 swz = (u32)(nl & 7) << 1;
  const lptr aB0 = stg + (nl * 256 + ((q ^ swz) << 4)), aB1 = stg + (nl * 256 + (((4 + q) ^ swz) << 4));
  lptr aV[4];
  {
    const u32 row = 4 * q + qq, sw = (row & 7) << 1;
#pragma unroll
    for (int cb = 0; cb < 4; ++cb) aV[cb] = stg + (row * 256 + (((8 + 2 * cb + (p >> 1)) ^ sw) << 4) + (p & 1) * 8);
  }
  const lptr aOH = ohr + (nl * 32 + q * 8), aS = ohr + lane * 8, aI = idr + q * 2;
  const u32 voffA = ((u32)nl ^ ((u32)q << 1)) << 4, voffB = ((u32)nl ^ ((u32)(4 + q) << 1)) << 4;
  u32x4 rsrc;
  {
    const u64 tb = (u64)(uintptr_t)(T + (size_t)slice * N * 256);
    rsrc[0] = (u32)tb; rsrc[1] = ((u32)(tb >> 32) & 0xffffu) | (256u << 16); rsrc[2] = (u32)N; rsrc[3] = 0x00020000u;
  }
  s16x4 ident;
#pragma unroll
  for (int j = 0; j < 4; ++j) ident[j] = (4 * q + j == nl) ? (short)0x3f80 : (short)0;

  f32x4 Yd[4], Yn[4];
  u32x2 Uv[4];
  s16x4 AW[4];
#pragma unroll
  for (int cb = 0; cb < 4; ++cb) { Yd[cb] = f32x4{0.f, 0.f, 0.f, 0.f}; Yn[cb] = f32x4{0.f, 0.f, 0.f, 0.f}; }
  const f32x4 cinit = {cneg, cneg, cneg, cneg};
  const f32x2 cadd2 = {cadd, cadd};

  u32x3 raw[6];
  auto setup = [&](int w0) {
    const int nw = min(ntot - w0, WIN);
    const u32 c0 = o0 >> 16, c1 = o1 >> 16;
    const u32 pre0 = wave_scan_add(c0 + c1) - (c0 + c1), pre1 = pre0 + c0;
    const u32 dl0 = (u32)lane * (u32)CH + (o0 & 0xffffu) - pre0, dl1 = (u32)(lane + 64) * (u32)CH + (o1 & 0xffffu) - pre1;
    const lptr mk = ohr, dt = ohr + WIN * 4;
#pragma unroll
    for (int g = 0; g < 6; ++g) lds_st<u32>(mk + lane * 4 + g * 256, 0u);
    lds_st<u32x2>(dt + lane * 8, u32x2{dl0, dl1});
    __builtin_amdgcn_wave_barrier();
    if (c0 && (int)(pre0 + c0) > w0 && (int)pre0 < w0 + WIN) lds_st<u32>(mk + max((int)pre0 - w0, 0) * 4, (u32)(2 * lane + 1));
    if (c1 && (int)(pre1 + c1) > w0 && (int)pre1 < w0 + WIN) lds_st<u32>(mk + max((int)pre1 - w0, 0) * 4, (u32)(2 * lane + 2));
    __builtin_amdgcn_wave_barrier();
    u32 carry = 0;
#pragma unroll
    for (int g = 0; g < 6; ++g) {
      raw[g] = u32x3{0u, 0u, 15u << 28};
      if (g * 64 < nw) {
        u32 id = lds_ld<u32>(mk + lane * 4 + g * 256);
        id = umax(wave_scan_max(id), carry);
        carry = __builtin_amdgcn_readlane(id, 63);
        const int j = g * 64 + lane;
        const u32 d = lds_ld<u32>(dt + (id - 1) * 4);
        if (j < nw) raw[g] = *(const u32x3*)(rec + (size_t)(u32)(w0 + j + d) * 3);
      }
    }
    __builtin_amdgcn_wave_barrier();
  };
  auto expand = [&](const u32x3& r, int half) {
    const lptr a = ohr + lane * 32 + half * 2048;
    lds_st<u32x4>(a, u32x4{0u, 0u, 0u, 0u});
    lds_st<u32x4>(a + 16, u32x4{0u, 0u, r[0], r[1]});
    const u32 dl = r[2] >> 28;
    if (dl < 12) lds_st<u16>(a + dl * 2, (u16)0x3f80);
    lds_st<u16>(idr + lane * 2 + half * 128, (u16)r[2]);
  };
  STAMP(1);
  setup(0);
  STAMP(2);
  asm volatile("s_waitcnt vmcnt(0)" ::: "memory");
  STAMP(3);
  __builtin_amdgcn_s_barrier();
#pragma unroll
  for (int cb = 0; cb < 4; ++cb) {
    f32x4 aa = {0.f, 0.f, 0.f, 0.f}, uu = {0.f, 0.f, 0.f, 0.f};
    const int byte = 32 * p + 8 * cb;
#pragma unroll
    for (int ks = 0; ks < 4; ++ks) {
      s16x4 ha[2], hu[2];
#pragma unroll
      for (int hh = 0; hh < 2; ++hh) {
        const int k = 32 * ks + 8 * q + 4 * hh + qq;
        const lptr a = L + (k * 128 + (((byte >> 4) ^ ((k >> 1) & 1)) << 4) + (byte & 8));
        ha[hh] = lds_tr(a);
        hu[hh] = lds_tr(a + 16384);
      }
      const s16x8 fa = __builtin_shufflevector(ha[0], ha[1], 0, 1, 2, 3, 4, 5, 6, 7);
      const s16x8 fu = __builtin_shufflevector(hu[0], hu[1], 0, 1, 2, 3, 4, 5, 6, 7);
      aa = __builtin_amdgcn_mfma_f32_16x16x32_bf16(xf[ks], fa, aa, 0, 0, 0);
      uu = __builtin_amdgcn_mfma_f32_16x16x32_bf16(fu, xf[ks], uu, 0, 0, 0);
    }
    u32x2 aw = {pk_bf16(aa[0] * NLOG2E, aa[1] * NLOG2E), pk_bf16(aa[2] * NLOG2E, aa[3] * NLOG2E)};
    if (q == 3) aw = wcv[cb];
    AW[cb] = __builtin_bit_cast(s16x4, aw);
    Uv[cb] = u32x2{pk_bf16(uu[0], uu[1]), pk_bf16(uu[2], uu[3])};
    asm volatile("" : "+v"(Uv[cb]), "+v"(AW[cb]));
  }
  asm volatile("s_waitcnt lgkmcnt(0)" ::: "memory");
  __builtin_amdgcn_s_barrier();
  STAMP(4);
#pragma nounroll
  for (int w0 = 0; w0 < ntot; w0 += WIN) {
    if (w0) { asm volatile("" : "+v"(o0), "+v"(o1)); setup(w0); }
    const int nw = min(ntot - w0, WIN);
    asm volatile("" : "+v"(raw[0]), "+v"(raw[1]), "+v"(raw[2]), "+v"(raw[3]), "+v"(raw[4]), "+v"(raw[5]));
    expand(raw[0], 0);
    expand(raw[1], 1);
    const int nt = (nw + 15) >> 4;
    auto issue = [&](int rt, int slot) {
      u32 id[4];
#pragma unroll
      for (int k = 0; k < 4; ++k) id[k] = lds_ld<u16>(aI + rt * 32 + k * 8);
      asm volatile("s_waitcnt lgkmcnt(0)" ::: "memory");
#pragma unroll
      for (int k = 0; k < 4; ++k) dma_b(stgm0 + slot * 4096 + k * 1024, rsrc, u32x2{id[k], (k & 1) ? voffB : voffA});
    };
    if (nt > 0) issue(0, 0);
    if (nt > 1) issue(1, 1);
#define TILE_STEP(RT)                                                                                              \
    if (i0 + (RT) < nt) {                                                                                          \
      constexpr int SL = (RT) & 1;                                                                                 \
      const int i = i0 + (RT);                                                                                     \
      if (i + 1 < nt) asm volatile("s_waitcnt vmcnt(4)" ::: "memory");                                             \
      else asm volatile("s_waitcnt vmcnt(0)" ::: "memory");                                                        \
      const s16x8 b0 = lds_ld<s16x8>(aB0 + SL * 4096), b1 = lds_ld<s16x8>(aB1 + SL * 4096);                        \
      const s16x4 oh = lds_ld<s16x4>(aOH + (RT) * 512);                                                            \
      s16x4 vv[4];                                                                                                 \
      _Pragma("unroll") for (int cb = 0; cb < 4; ++cb) vv[cb] = lds_tr(aV[cb] + SL * 4096);                        \
      const h16x4 sm = __builtin_bit_cast(h16x4, lds_tr(aS + (RT) * 512));                                         \
      if (i + 2 < nt) issue(((RT) + 2) & 7, SL);                                                                   \
      f32x4 t[4];                                                                                                  \
      _Pragma("unroll") for (int cb = 0; cb < 4; ++cb)                                                             \
        t[cb] = __builtin_amdgcn_mfma_f32_16x16x16bf16_1k(oh, AW[cb], cinit, 0, 0, 0);                             \
      _Pragma("unroll") for (int cb = 0; cb < 4; ++cb) {                                                           \
        const s16x8 bb = (cb & 2) ? b1 : b0;                                                                       \
        const s16x4 bf = (cb & 1) ? __builtin_shufflevector(bb, bb, 4, 5, 6, 7) : __builtin_shufflevector(bb, bb, 0, 1, 2, 3); \
        t[cb] = __builtin_amdgcn_mfma_f32_16x16x16bf16_1k(bf, ident, t[cb], 0, 0, 0);                              \
      }                                                                                                            \
      _Pragma("unroll") for (int cb = 0; cb < 4; ++cb) {                                                           \
        f32x2 d0 = {__builtin_amdgcn_exp2f(t[cb][0]), __builtin_amdgcn_exp2f(t[cb][1])};                           \
        f32x2 d1 = {__builtin_amdgcn_exp2f(t[cb][2]), __builtin_amdgcn_exp2f(t[cb][3])};                           \
        d0 += cadd2; d1 += cadd2;                                                                                  \
        const h16x2 e0 = __builtin_bit_cast(h16x2, __builtin_amdgcn_cvt_pkrtz(__builtin_amdgcn_rcpf(d0[0]), __builtin_amdgcn_rcpf(d0[1]))); \
        const h16x2 e1 = __builtin_bit_cast(h16x2, __builtin_amdgcn_cvt_pkrtz(__builtin_amdgcn_rcpf(d1[0]), __builtin_amdgcn_rcpf(d1[1]))); \
        const h16x4 e = __builtin_shufflevector(e0, e1, 0, 1, 2, 3);                                               \
        const h16x4 pr = e * __builtin_bit_cast(h16x4, vv[cb]);                                                    \
        Yd[cb] = __builtin_amdgcn_mfma_f32_16x16x16f16(e, sm, Yd[cb], 0, 0, 0);                                    \
        Yn[cb] = __builtin_amdgcn_mfma_f32_16x16x16f16(pr, sm, Yn[cb], 0, 0, 0);                                   \
      }                                                                                                            \
      if ((RT) == 3) { if (i == 3) expand(raw[2], 0); else if (i == 11) expand(raw[4], 0); }                       \
      if ((RT) == 7) { if (i == 7) expand(raw[3], 1); else if (i == 15) expand(raw[5], 1); }                       \
    }
#pragma nounroll
    for (int i0 = 0; i0 < nt; i0 += 8) {
      TILE_STEP(0) TILE_STEP(1) TILE_STEP(2) TILE_STEP(3) TILE_STEP(4) TILE_STEP(5) TILE_STEP(6) TILE_STEP(7)
    }
#undef TILE_STEP
  }
  STAMP(5);
  if (nl < nb) {
    const size_t o = (size_t)(n0 + nl) * 128 + slice * 64 + q * 16;
    const u32x4 x0 = *(const u32x4*)(x + o), x1 = *(const u32x4*)(x + o + 8);
    u32x4 r0, r1;
#pragma unroll
    for (int cb = 0; cb < 4; ++cb) {
      float z[4];
#pragma unroll
      for (int i = 0; i < 4; ++i) {
        const float agg = Yn[cb][i] * __builtin_amdgcn_rcpf(Yd[cb][i] + epsv);
        const float zz = ((i & 1) ? bf_hi(Uv[cb][i >> 1]) : bf_lo(Uv[cb][i >> 1])) + agg;
        z[i] = zz > 0.f ? zz : 0.f * zz;
      }
      const u32 xa = (cb & 2) ? x1[(cb & 1) * 2] : x0[(cb & 1) * 2], xb = (cb & 2) ? x1[(cb & 1) * 2 + 1] : x0[(cb & 1) * 2 + 1];
      const u32 ra = pk_bf16(bf_lo(xa) + z[0], bf_hi(xa) + z[1]), rb = pk_bf16(bf_lo(xb) + z[2], bf_hi(xb) + z[3]);
      if (cb & 2) { r1[(cb & 1) * 2] = ra; r1[(cb & 1) * 2 + 1] = rb; } else { r0[(cb & 1) * 2] = ra; r0[(cb & 1) * 2 + 1] = rb; }
    }
    *(u32x4*)(out + o) = r0;
    *(u32x4*)(out + o + 8) = r1;
  }
  STAMP(6);
  STAMP_OUT(blockIdx.x * 4 + w);
}

extern "C" void kernel_launch(void* const* d_in, const int* in_sizes, int n_in, void* d_out, int out_size, void* d_ws,
                              size_t ws_size, hipStream_t stream) {
  const int N = in_sizes[0] / 128, E = in_sizes[2];
  int Bk = 1536;
  if (N > 12 * Bk) Bk = ((N + 11) / 12 + 15) / 16 * 16;
  const int CH = (E + NCH - 1) / NCH;
  const int G = 128;
  int RPG = (N + G - 1) / G;
  int Gn = G;
  if (RPG > GBLK * 16) { RPG = GBLK * 16; Gn = (N + RPG - 1) / RPG; }
  char* ws = (char*)d_ws;
  size_t o = 0;
  char* T = ws + o; o += (size_t)2 * N * 256; o = (o + 255) & ~(size_t)255;
  u32* rec = (u32*)(ws + o); o += (size_t)NCH * CH * 12; o = (o + 255) & ~(size_t)255;
  u32* off = (u32*)(ws + o); o += (size_t)NCH * Bk * 4; o = (o + 255) & ~(size_t)255;
  u32* vmax = (u32*)(ws + o); o += (size_t)Gn * 4; o = (o + 255) & ~(size_t)255;
  u16* wcp = (u16*)(ws + o);
  const u64 magN = ((1ull << 48) + (u64)N - 1) / (u64)N, magB = ((1ull << 48) + (u64)Bk - 1) / (u64)Bk;
  const int lds1 = 65536 + GBLK * 16 * 256 + 16;
  (void)hipFuncSetAttribute((const void*)k_prep, hipFuncAttributeMaxDynamicSharedMemorySize, lds1);
  k_prep<<<NCH + Gn, K1T, lds1, stream>>>((const u16*)d_in[0], (const u16*)d_in[1], (const int*)d_in[2], (const int*)d_in[3],
                                         (const u16*)d_in[5], (const u16*)d_in[6], (const u16*)d_in[8], T, rec, off, vmax, wcp,
                                         magN, magB, N, E, Bk, CH, RPG);
  k_edge<<<Bk / 4 * 2, 256, K2LDS, stream>>>((const u16*)d_in[0], (const u16*)d_in[4], (const u16*)d_in[7], wcp, T, rec, off, vmax,
                                            (u16*)d_out, magB, N, Bk, CH, Gn);
}
```

```cpp
#include <hip/hip_runtime.h>
#include <stdint.h>

typedef unsigned int u32;
typedef unsigned short u16;
typedef unsigned long long u64;
using s16x4 = __attribute__((ext_vector_type(4))) short;
using s16x8 = __attribute__((ext_vector_type(8))) short;
using h16x4 = __attribute__((ext_vector_type(4))) _Float16;
using h16x2 = __attribute__((ext_vector_type(2))) _Float16;
using b16x2 = __attribute__((ext_vector_type(2))) __bf16;
using f32x4 = __attribute__((ext_vector_type(4))) float;
using f32x2 = __attribute__((ext_vector_type(2))) float;
using u32x2 = __attribute__((ext_vector_type(2))) u32;
using u32x3 = __attribute__((ext_vector_type(3))) u32;
using u32x4 = __attribute__((ext_vector_type(4))) u32;
#define LAS __attribute__((address_space(3)))
#define DI __device__ __forceinline__

constexpr int NCH = 64;
constexpr int EPT = 13;
constexpr int K1T = 512;
constexpr int GBLK = 7;
constexpr float NLOG2E = -1.4426950408889634f;
constexpr int NG = 5;
constexpr int WIN = NG * 64;

typedef LAS char* lptr;
template <class T> DI T lds_ld(lptr a) { return *(LAS T*)a; }
template <class T> DI void lds_st(lptr a, T v) { *(LAS T*)a = v; }
DI s16x4 lds_tr(lptr a) { return __builtin_amdgcn_ds_read_tr16_b64_v4i16((LAS s16x4*)a); }
DI u32 pk_bf16(float lo, float hi) { f32x2 v = {lo, hi}; return __builtin_bit_cast(u32, __builtin_convertvector(v, b16x2)); }
DI float bf_lo(u32 w) { return __uint_as_float(w << 16); }
DI float bf_hi(u32 w) { return __uint_as_float(w & 0xffff0000u); }

DI void dma_g(u32 m0v, const void* base, u32 voff) {
  asm volatile("s_mov_b32 m0, %0\n\ts_nop 0\n\tglobal_load_lds_dwordx4 %1, %2" ::"s"(m0v), "v"(voff), "s"(base) : "memory");
}
DI void dma_b(u32 m0v, u32x4 rsrc, u32x2 io) {
  asm volatile("s_mov_b32 m0, %0\n\ts_nop 0\n\tbuffer_load_dwordx4 %1, %2, 0 idxen offen lds" ::"s"(m0v), "v"(io), "s"(rsrc) : "memory");
}

#define STP 0
DI void gst4(void* p, u32x4 v) {
  *(u32x4*)p = v;
}
template <int CTRL, int RM> DI u32 dpp0(u32 v) { return (u32)__builtin_amdgcn_update_dpp(0, (int)v, CTRL, RM, 0xf, false); }
DI u32 wave_scan_add(u32 v) {
  v += dpp0<0x111, 0xf>(v); v += dpp0<0x112, 0xf>(v); v += dpp0<0x114, 0xf>(v); v += dpp0<0x118, 0xf>(v);
  v += dpp0<0x142, 0xa>(v); v += dpp0<0x143, 0xc>(v);
  return v;
}
DI u32 umax(u32 a, u32 b) { return a > b ? a : b; }
DI u32 wave_scan_max(u32 v) {
  v = umax(v, dpp0<0x111, 0xf>(v)); v = umax(v, dpp0<0x112, 0xf>(v)); v = umax(v, dpp0<0x114, 0xf>(v)); v = umax(v, dpp0<0x118, 0xf>(v));
  v = umax(v, dpp0<0x142, 0xa>(v)); v = umax(v, dpp0<0x143, 0xc>(v));
  return v;
}
DI u32 divm(u32 x, u64 magic) { return (u32)(((u64)x * magic) >> 48); }

#define STAMP(k)
#define STAMP_DECL
#define HWID
#define CYC0
#define CYC1
#define STAMP_OUT(id)
__global__ __launch_bounds__(K1T) void k_prep(const u16* __restrict__ x, const u16* __restrict__ ea, const int* __restrict__ src,
                                             const int* __restrict__ dst, const u16* __restrict__ wa, const u16* __restrict__ wb,
                                             const u16* __restrict__ wc, const u16* __restrict__ wu, const u16* __restrict__ wv,
                                             char* __restrict__ T, char* __restrict__ AU, u32* __restrict__ rec,
                                             u32* __restrict__ off, u32* __restrict__ vmax, u16* __restrict__ wcp, u64 magN,
                                             u64 magB, int N, int E, int Bk, int CH, int GW) {
  extern __shared__ __attribute__((aligned(16))) char smem[];
  const lptr L = (lptr)smem;
  const u32 lb = (u32)(uintptr_t)L;
  const int tid = threadIdx.x, lane = tid & 63;
  const int w = __builtin_amdgcn_readfirstlane(tid >> 6);
  STAMP_DECL; STAMP(0);
  if ((int)blockIdx.x < NCH) {
    const int c = blockIdx.x;
    const int e0 = c * CH, e1 = min(E, e0 + CH);
    u32* hist = (u32*)smem;
    u32* wsum = hist + Bk / 2;
    u32* stage = wsum + 16;
    int dv[EPT], sv[EPT];
    u32x2 ev[EPT];
#pragma unroll
    for (int i = 0; i < EPT; ++i) { const int e = e0 + tid + i * K1T; dv[i] = dst[e < e1 ? e : 0]; }
    for (int i = tid; i < Bk / 2; i += K1T) hist[i] = 0;
    asm volatile("s_waitcnt lgkmcnt(0)" ::: "memory");
    __builtin_amdgcn_s_barrier();
    STAMP(1);
    u32 br[EPT];
#pragma unroll
    for (int i = 0; i < EPT; ++i) {
      const int e = e0 + tid + i * K1T;
      const u32 bk = divm((u32)(dv[i] + 1) * (u32)Bk - 1u, magN);
      const u32 sh = (bk & 1u) << 4;
      u32 r = 0;
      if (e < e1) r = atomicAdd(&hist[bk >> 1], 1u << sh);
      br[i] = bk | ((r >> sh) << 16);
    }
    asm volatile("" ::: "memory");
#pragma unroll
    for (int i = 0; i < EPT; ++i) {
      const int e = e0 + tid + i * K1T, ee = e < e1 ? e : 0;
      sv[i] = src[ee]; ev[i] = *(const u32x2*)(ea + (size_t)ee * 4);
    }
    asm volatile("s_waitcnt lgkmcnt(0)" ::: "memory");
    __builtin_amdgcn_s_barrier();
    STAMP(2);
    u32 carry = 0;
    for (int sb0 = 0; sb0 < Bk / 4; sb0 += K1T) {
      const int sbi = sb0 + tid;
      u32x2 cn = {0u, 0u};
      if (sbi < Bk / 4) cn = *(const u32x2*)(hist + sbi * 2);
      const u32 k0 = cn[0] & 0xffffu, k1 = cn[0] >> 16, k2 = cn[1] & 0xffffu, k3 = cn[1] >> 16;
      const u32 loc = k0 + k1 + k2 + k3;
      const u32 inc = wave_scan_add(loc);
      if (lane == 63) wsum[w] = inc;
      asm volatile("s_waitcnt lgkmcnt(0)" ::: "memory");
      __builtin_amdgcn_s_barrier();
      const u32x4 s0 = *(const u32x4*)wsum, s1 = *(const u32x4*)(wsum + 4);
      const u32 base = carry + inc - loc + (w > 0 ? s0[0] : 0u) + (w > 1 ? s0[1] : 0u) + (w > 2 ? s0[2] : 0u) + (w > 3 ? s0[3] : 0u) +
                       (w > 4 ? s1[0] : 0u) + (w > 5 ? s1[1] : 0u) + (w > 6 ? s1[2] : 0u);
      carry += s0[0] + s0[1] + s0[2] + s0[3] + s1[0] + s1[1] + s1[2] + s1[3];
      if (sbi < Bk / 4) {
        const u32 b1 = base + k0, b2 = b1 + k1, b3 = b2 + k2;
        *(u32x2*)(hist + sbi * 2) = u32x2{base | (b1 << 16), b2 | (b3 << 16)};
        *(u32x4*)(off + ((size_t)sbi * NCH + (c & 7) * (NCH / 8) + (c >> 3)) * 4) = u32x4{base | (k0 << 16), b1 | (k1 << 16), b2 | (k2 << 16), b3 | (k3 << 16)};
      }
      asm volatile("s_waitcnt lgkmcnt(0)" ::: "memory");
      __builtin_amdgcn_s_barrier();
    }
    STAMP(3);
#pragma unroll
    for (int i = 0; i < EPT; ++i) {
      const int e = e0 + tid + i * K1T;
      if (e < e1) {
        const u32 bk = br[i] & 0xffffu;
        const u32 pos = (u32)((const u16*)hist)[bk] + (br[i] >> 16);
        const u32 dl = (u32)dv[i] - divm(bk * (u32)N, magB);
        u32* d = stage + pos * 3;
        d[0] = ev[i][0]; d[1] = ev[i][1]; d[2] = (u32)sv[i] | (dl << 24);
      }
    }
    asm volatile("s_waitcnt lgkmcnt(0)" ::: "memory");
    __builtin_amdgcn_s_barrier();
    STAMP(4);
    {
      const int nq = ((e1 - e0) * 3 + 3) >> 2;
      u32x4* g = (u32x4*)(rec + (size_t)c * CH * 3);
      for (int i = tid; i < nq; i += K1T) gst4(g + i, *(const u32x4*)(stage + i * 4));
    }
    STAMP(5);
    STAMP_OUT(4096 + blockIdx.x * 8 + w);
    return;
  }
  const int g = blockIdx.x - NCH;
  const int mp = (g >> 3) & 1, gi = (g & 7) + 8 * (g >> 4);
  const int q = lane >> 4, qq = (lane >> 2) & 3, p = lane & 3, nl = lane & 15;
  const u32 MO = 65536;
  const int nblk = (N + 15) >> 4, nwv = GW * 8;
  u32 vm = 0;
  char* dbase = mp ? AU : T;
  bool first = true;
  for (int blk = gi * 8 + w; first || blk < nblk; blk += nwv) {
    s16x8 xf[4];
    {
      const int row = min(blk * 16 + nl, N - 1);
#pragma unroll
      for (int ks = 0; ks < 4; ++ks) xf[ks] = *(const s16x8*)(x + (size_t)row * 128 + ks * 32 + q * 8);
    }
    if (first) {
      const u16* W0 = mp ? wa : wb;
      const u16* W1 = mp ? wu : wv;
      const int pos = lane & 15;
#pragma unroll
      for (int i = 0; i < 8; ++i) {
        const int m = w * 8 + i;
        const int k = (m & 31) * 4 + q;
        const int fw = (k & 1) | ((k & 2) << 2);
        dma_g(lb + m * 1024, (m >> 5) ? (const void*)W1 : (const void*)W0, (u32)(k * 256 + ((pos ^ fw) << 4)));
      }
      if (tid == 0) lds_st<u32>(L + MO, 0u);
      if (g == 0) {
        const int s = tid >> 8, j = (tid >> 6) & 3, cb = (tid >> 4) & 3, n = tid & 15;
        const int lam = 16 * (n >> 2) + 4 * cb + (n & 3);
        const float v = __uint_as_float((u32)wc[j * 128 + 64 * s + lam] << 16) * NLOG2E;
        wcp[tid] = (u16)(pk_bf16(v, 0.f) & 0xffffu);
      }
      STAMP(1);
      asm volatile("s_waitcnt vmcnt(0) lgkmcnt(0)" ::: "memory");
      __builtin_amdgcn_s_barrier();
      STAMP(2);
      first = false;
    }
    const int node = blk * 16 + nl;
    const bool valid = node < N;
#pragma unroll
    for (int mi = 0; mi < 2; ++mi) {
#pragma unroll
      for (int P = 0; P < 4; ++P) {
        const int s = P >> 1, kq = P & 1;
        f32x4 ac[2];
#pragma unroll
        for (int be = 0; be < 2; ++be) {
          const int lamB = 16 * p + 8 * kq + 4 * be, lamU = 32 * kq + 8 * p + 4 * be, lamV = 16 * (2 * (p & 1) + be) + 4 * (2 * kq + (p >> 1));
          const int lam0 = mi == 0 ? (mp ? lamV : lamB) : (mp ? lamU : lamV);
          const int byte = 2 * (64 * s + lam0);
          ac[be] = f32x4{0.f, 0.f, 0.f, 0.f};
#pragma unroll
          for (int ks = 0; ks < 4; ++ks) {
            s16x4 h[2];
#pragma unroll
            for (int hh = 0; hh < 2; ++hh) {
              const int k = 32 * ks + 8 * q + 4 * hh + qq;
              const int fw = (k & 1) | ((k & 2) << 2);
              h[hh] = lds_tr(L + (mi * 32768 + k * 256 + (((byte >> 4) ^ fw) << 4) + (byte & 8)));
            }
            ac[be] = __builtin_amdgcn_mfma_f32_16x16x32_bf16(__builtin_shufflevector(h[0], h[1], 0, 1, 2, 3, 4, 5, 6, 7), xf[ks], ac[be], 0, 0, 0);
          }
        }
        const f32x4 a0 = ac[0], a1 = ac[1];
        u32x4 o;
        if (mi == 1 && mp == 0) {
          h16x2 h0 = {(_Float16)a0[0], (_Float16)a0[1]}, h1 = {(_Float16)a0[2], (_Float16)a0[3]};
          h16x2 h2 = {(_Float16)a1[0], (_Float16)a1[1]}, h3 = {(_Float16)a1[2], (_Float16)a1[3]};
          o[0] = __builtin_bit_cast(u32, h0); o[1] = __builtin_bit_cast(u32, h1);
          o[2] = __builtin_bit_cast(u32, h2); o[3] = __builtin_bit_cast(u32, h3);
#pragma unroll
          for (int i = 0; i < 4; ++i) { vm = umax(vm, __float_as_uint(a0[i]) & 0x7fffffffu); vm = umax(vm, __float_as_uint(a1[i]) & 0x7fffffffu); }
        } else {
          const float scl = mi == 0 ? NLOG2E : 1.f;
          o[0] = pk_bf16(a0[0] * scl, a0[1] * scl); o[1] = pk_bf16(a0[2] * scl, a0[3] * scl);
          o[2] = pk_bf16(a1[0] * scl, a1[1] * scl); o[3] = pk_bf16(a1[2] * scl, a1[3] * scl);
        }
        if (valid) gst4(dbase + ((size_t)s * N + node) * 256 + mi * 128 + (4 * kq + q) * 16, o);
      }
    }
  }
  STAMP(4);
  STAMP(5);
  STAMP_OUT(4096 + blockIdx.x * 8 + w);
  if (mp == 0) {
    const u32 m = wave_scan_max(vm);
    if (lane == 63) atomicMax((u32*)(smem + MO), m);
    asm volatile("s_waitcnt lgkmcnt(0)" ::: "memory");
    __builtin_amdgcn_s_barrier();
    if (tid == 0) vmax[gi] = lds_ld<u32>(L + MO);
  }
}

#define EXP_ID(x) (x)
constexpr u32 STG = 8192;
constexpr u32 OHR = 4096, IDR = 512;
constexpr u32 K2LDS = 4 * STG + 4 * (OHR + IDR) + 1024;

__global__ __launch_bounds__(256, 3) void k_edge(const u16* __restrict__ x, const u16* __restrict__ wcp, const char* __restrict__ T,
                                                const char* __restrict__ AU, const u32* __restrict__ rec,
                                                const u32* __restrict__ off, const u32* __restrict__ vmax,
                                                u16* __restrict__ out, u64 magB, int N, int Bk, int CH, int nvm) {
  extern __shared__ __attribute__((aligned(16))) char smem[];
  const lptr L = (lptr)smem;
  const u32 lb = (u32)(uintptr_t)L;
  const int tid = threadIdx.x, lane = tid & 63;
  const int w = __builtin_amdgcn_readfirstlane(tid >> 6);
  STAMP_DECL; STAMP(0);
  const int r8 = blockIdx.x & 7, slice = r8 & 1;
  const int sb = (blockIdx.x >> 3) * 4 + (r8 >> 1);
  const int b = sb * 4 + w;
  const int q = lane >> 4, nl = lane & 15, qq = (lane >> 2) & 3, p = lane & 3;
  const lptr stg = L + w * STG, ohr = L + 4 * STG + w * (OHR + IDR), idr = ohr + OHR;
  const u32 stgm0 = lb + w * STG;

  const int n0 = (int)divm((u32)b * (u32)N, magB), n1 = (int)divm((u32)(b + 1) * (u32)N, magB);
  const int nb = n1 - n0;
  const char* Ts = T + (size_t)slice * N * 256;
  const char* AUs = AU + (size_t)slice * N * 256;
  const int chunk = (lane & 7) * (NCH / 8) + (lane >> 3);
  const u32x4 o4 = *(const u32x4*)(off + ((size_t)sb * NCH + lane) * 4);
  const u32 cid = (u32)((lane % (NCH / 8)) * 8 + lane / (NCH / 8));
  (void)chunk;
  {
    const int rr = lane >> 3, pos = lane & 7;
    const int r0 = min(n0 + min(rr, max(nb - 1, 0)), N - 1), r1 = min(n0 + min(8 + rr, max(nb - 1, 0)), N - 1);
    const u32 awm0 = lb + 4 * STG + w * (OHR + IDR) + 2048;
    dma_g(awm0, AUs, (u32)(r0 * 256 + pos * 16));
    const char* s1 = rr < 4 ? AUs + (size_t)(r1 * 256 + pos * 16) : (const char*)wcp + (slice * 512 + (rr - 4) * 128 + pos * 16);
    asm volatile("s_mov_b32 m0, %0\n\ts_nop 0\n\tglobal_load_lds_dwordx4 %1, off" ::"s"(awm0 + 1024), "v"(s1) : "memory");
  }
  u32 vmx = 0;
  for (int i = lane; i < nvm; i += 64) vmx = umax(vmx, vmax[i]);

  const u32 swz = (u32)(nl & 7) << 1;
  lptr aB0 = stg + (nl * 256 + ((q ^ swz) << 4)), aB1 = stg + (nl * 256 + (((4 + q) ^ swz) << 4));
  lptr aV[4];
  {
    const u32 row = 4 * q + qq, sw = (row & 7) << 1;
#pragma unroll
    for (int cb = 0; cb < 4; ++cb) aV[cb] = stg + (row * 256 + (((8 + 2 * cb + (p >> 1)) ^ sw) << 4) + (p & 1) * 8);
  }
  lptr aOH = ohr + (nl * 32 + q * 8), aS = ohr + lane * 8, aI = idr + q * 2;
  u32 voff[4];
  voff[0] = voff[2] = ((u32)nl ^ ((u32)q << 1)) << 4; voff[1] = voff[3] = ((u32)nl ^ ((u32)(4 + q) << 1)) << 4;
  u32x4 rsrc;
  {
    const u64 tbp = (u64)(uintptr_t)Ts;
    rsrc[0] = (u32)tbp; rsrc[1] = ((u32)(tbp >> 32) & 0xffffu) | (256u << 16); rsrc[2] = (u32)N; rsrc[3] = 0x00020000u;
  }
  s16x4 ident;
#pragma unroll
  for (int j = 0; j < 4; ++j) ident[j] = (4 * q + j == nl) ? (short)0x3f80 : (short)0;
  f32x4 Yd[4], Yn[4];
#pragma unroll
  for (int cb = 0; cb < 4; ++cb) { Yd[cb] = f32x4{0.f, 0.f, 0.f, 0.f}; Yn[cb] = f32x4{0.f, 0.f, 0.f, 0.f}; }
  asm volatile("" : "+v"(aB0), "+v"(aB1), "+v"(aV[0]), "+v"(aV[1]), "+v"(aV[2]), "+v"(aV[3]), "+v"(aOH), "+v"(aS), "+v"(aI));
  asm volatile("" : "+v"(voff[0]), "+v"(voff[1]), "+v"(voff[2]), "+v"(voff[3]), "+v"(ident));
  asm volatile("" : "+v"(Yd[0]), "+v"(Yd[1]), "+v"(Yd[2]), "+v"(Yd[3]), "+v"(Yn[0]), "+v"(Yn[1]), "+v"(Yn[2]), "+v"(Yn[3]));

  const u32 gst = cid * (u32)CH + (o4[0] & 0xffffu);
  const u32 rlen = (o4[3] & 0xffffu) + (o4[3] >> 16) - (o4[0] & 0xffffu);
  {
    const u32 l31 = (u32)lane & 31u;
#pragma unroll
    for (int m = 0; m < 8; ++m) {
      const u32 ga = __builtin_amdgcn_readlane(gst, w * 16 + 2 * m), gb = __builtin_amdgcn_readlane(gst, w * 16 + 2 * m + 1);
      const u32 la = __builtin_amdgcn_readlane(rlen, w * 16 + 2 * m), lb2 = __builtin_amdgcn_readlane(rlen, w * 16 + 2 * m + 1);
      const u32 vo = ((lane < 32 ? ga : gb) + l31) * 12u;
      if (l31 < (lane < 32 ? la : lb2))
        asm volatile("s_mov_b32 m0, %0\n\ts_nop 0\n\tglobal_load_lds_dwordx3 %1, %2" ::"s"(lb + (w * 16 + 2 * m) * 512), "v"(vo), "s"(rec) : "memory");
    }
  }
  STAMP(1);
  vmx = __builtin_amdgcn_readlane(wave_scan_max(vmx), 63);
  int ea_ = 15 - ((int)(vmx >> 23) - 126);
  ea_ = ea_ > 15 ? 15 : ea_;
  const float cneg = (float)(-ea_), cadd = __uint_as_float((u32)(127 - ea_) << 23);
  const float epsv = 1e-6f * 1.875f * __uint_as_float((u32)(127 + ea_) << 23);
  f32x4 cinit = {cneg, cneg, cneg, cneg};
  const f32x2 cadd2 = {cadd, cadd};
  asm volatile("" : "+v"(cinit));
  u32 o0 = w == 0 ? o4[0] : w == 1 ? o4[1] : w == 2 ? o4[2] : o4[3];
  u32x3 raw[NG];
  const u32x3 padrec = {0u, 0u, 15u << 24};
  const u32 inc0 = wave_scan_add(o0 >> 16);
  const int ntot = (int)__builtin_amdgcn_readlane(inc0, 63);
  const bool fast = ntot <= WIN;
  u32 gd0 = cid * (u32)CH + (o0 & 0xffffu) - (inc0 - (o0 >> 16));
  auto setup = [&](int w0) {
    const int nw = min(ntot - w0, WIN);
    const u32 c0 = o0 >> 16;
    const u32 pre0 = wave_scan_add(c0) - c0;
    const lptr mk = ohr, dt = ohr + 512;
    lds_st<u32x2>(mk + lane * 8, u32x2{0u, 0u});
    lds_st<u32>(dt + lane * 4, gd0);
    __builtin_amdgcn_wave_barrier();
    if (c0 && (int)(pre0 + c0) > w0 && (int)pre0 < w0 + WIN) lds_st<unsigned char>(mk + max((int)pre0 - w0, 0), (unsigned char)(lane + 1));
    __builtin_amdgcn_wave_barrier();
    u32 carry = 0;
#pragma unroll
    for (int g = 0; g < NG; ++g) {
      raw[g] = padrec;
      if (g * 64 < nw) {
        u32 id = lds_ld<unsigned char>(mk + lane + g * 64);
        id = umax(wave_scan_max(id), carry);
        carry = __builtin_amdgcn_readlane(id, 63);
        const int j = g * 64 + lane;
        const u32 d = lds_ld<u32>(dt + (id - 1) * 4);
        if (j < nw) raw[g] = *(const u32x3*)(rec + (size_t)(u32)(w0 + j + d) * 3);
      }
    }
    __builtin_amdgcn_wave_barrier();
  };
  u32x2 en[NG];
  if (fast) {
    const u32 c0 = o0 >> 16, pre0 = inc0 - c0;
    const u32 ra = (o0 & 0xffffu) - (o4[0] & 0xffffu);
    const lptr mk = ohr, dt = ohr + 512;
    lds_st<u32x2>(mk + lane * 8, u32x2{0u, 0u});
    const int lim = max(min((int)pre0 + 32 - (int)ra, 0x7fff), 0);
    lds_st<u32x2>(dt + lane * 8, u32x2{(((u32)lane * 32u + ra - pre0) & 0xffffu) | ((u32)lim << 16), gd0});
    __builtin_amdgcn_wave_barrier();
    if (c0) lds_st<unsigned char>(mk + pre0, (unsigned char)(lane + 1));
    __builtin_amdgcn_wave_barrier();
    u32 carry = 0;
#pragma unroll
    for (int g = 0; g < NG; ++g) {
      if (g * 64 < ntot) {
        u32 id = lds_ld<unsigned char>(mk + lane + g * 64);
        id = umax(wave_scan_max(id), carry);
        carry = __builtin_amdgcn_readlane(id, 63);
        en[g] = lds_ld<u32x2>(dt + (id - 1) * 8);
      }
    }
  }
  asm volatile("s_waitcnt vmcnt(0) lgkmcnt(0)" ::: "memory");
  __builtin_amdgcn_s_barrier();
  STAMP(2);
  s16x4 AW[4];
#pragma unroll
  for (int cb = 0; cb < 4; ++cb) AW[cb] = lds_tr(ohr + (2048 + (4 * q + qq) * 128 + cb * 32 + p * 8));
  if (fast) {
#pragma unroll
    for (int g = 0; g < NG; ++g) {
      raw[g] = padrec;
      if (g * 64 < ntot) {
        const int j = g * 64 + lane;
        const bool ovf = j >= (int)(en[g][0] >> 16);
        if (j < ntot) {
          if (!ovf) raw[g] = lds_ld<u32x3>(L + ((j + (int)(short)en[g][0]) << 4));
          else raw[g] = *(const u32x3*)(rec + (size_t)(u32)(j + en[g][1]) * 3);
        }
      }
    }
  }
  asm volatile("s_waitcnt lgkmcnt(0)" ::: "memory");
  __builtin_amdgcn_s_barrier();
  auto expand = [&](const u32x3& r, int half) {
    const lptr a = ohr + lane * 32 + half * 2048;
    lds_st<u32x4>(a, u32x4{0u, 0u, 0u, 0u});
    lds_st<u32x4>(a + 16, u32x4{0u, 0u, r[0], r[1]});
    const u32 dl = (r[2] >> 24) & 15u;
    if (dl < 12) lds_st<u16>(a + dl * 2, (u16)0x3f80);
    lds_st<u16>(idr + lane * 2 + half * 128, (u16)r[2]);
  };
  HWID;
  STAMP(4); CYC0;
#pragma nounroll
  for (int w0 = 0; w0 < ntot; w0 += WIN) {
    if (w0 || !fast) { asm volatile("" : "+v"(o0), "+v"(gd0)); setup(w0); }
    const int nw = min(ntot - w0, WIN);
    asm volatile("" : "+v"(raw[0]), "+v"(raw[1]), "+v"(raw[2]), "+v"(raw[3]), "+v"(raw[4]));
    expand(raw[0], 0);
    expand(raw[1], 1);
    const int nt = (nw + 15) >> 4;
    u32 id[4];
    auto idx_read = [&](int rt) {
#pragma unroll
      for (int k = 0; k < 4; ++k) id[k] = lds_ld<u16>(aI + rt * 32 + k * 8);
    };
    auto dma_go = [&](int slot) {
      asm volatile("s_waitcnt lgkmcnt(0)" ::: "memory");
#pragma unroll
      for (int k = 0; k < 4; ++k) dma_b(stgm0 + slot * 4096 + k * 1024, rsrc, u32x2{EXP_ID(id[k]), voff[k]});
    };
    s16x8 b0, b1;
    s16x4 oh, vv[4];
    h16x4 sm;
    idx_read(0); dma_go(0);
    if (nt > 1) { idx_read(1); dma_go(1); asm volatile("s_waitcnt vmcnt(4)" ::: "memory"); }
    else asm volatile("s_waitcnt vmcnt(0)" ::: "memory");
    b0 = lds_ld<s16x8>(aB0); b1 = lds_ld<s16x8>(aB1); oh = lds_ld<s16x4>(aOH);
#pragma unroll
    for (int cb = 0; cb < 4; ++cb) vv[cb] = lds_tr(aV[cb]);
    sm = __builtin_bit_cast(h16x4, lds_tr(aS));
    if (nt > 2) { idx_read(2); dma_go(0); }
#define GATE(cb)                                                                                                   \
      {                                                                                                            \
        f32x2 d0 = {__builtin_amdgcn_exp2f(t[cb][0]), __builtin_amdgcn_exp2f(t[cb][1])};                           \
        f32x2 d1 = {__builtin_amdgcn_exp2f(t[cb][2]), __builtin_amdgcn_exp2f(t[cb][3])};                           \
        d0 += cadd2; d1 += cadd2;                                                                                  \
        const h16x2 e0 = __builtin_bit_cast(h16x2, __builtin_amdgcn_cvt_pkrtz(__builtin_amdgcn_rcpf(d0[0]), __builtin_amdgcn_rcpf(d0[1]))); \
        const h16x2 e1 = __builtin_bit_cast(h16x2, __builtin_amdgcn_cvt_pkrtz(__builtin_amdgcn_rcpf(d1[0]), __builtin_amdgcn_rcpf(d1[1]))); \
        const h16x4 e = __builtin_shufflevector(e0, e1, 0, 1, 2, 3);                                               \
        const h16x4 pr = e * __builtin_bit_cast(h16x4, cv[cb]);                                                    \
        Yd[cb] = __builtin_amdgcn_mfma_f32_16x16x16f16(e, cs, Yd[cb], 0, 0, 0);                                    \
        Yn[cb] = __builtin_amdgcn_mfma_f32_16x16x16f16(pr, cs, Yn[cb], 0, 0, 0);                                   \
      }
#define TILE_STEP(RT)                                                                                              \
    if (i0 + (RT) < nt) {                                                                                          \
      constexpr int SN = ((RT) + 1) & 1, RN = ((RT) + 1) & 7;                                                      \
      const int i = i0 + (RT);                                                                                     \
      f32x4 t[4];                                                                                                  \
      _Pragma("unroll") for (int cb = 0; cb < 4; ++cb)                                                             \
        t[cb] = __builtin_amdgcn_mfma_f32_16x16x16bf16_1k(oh, AW[cb], cinit, 0, 0, 0);                             \
      _Pragma("unroll") for (int cb = 0; cb < 4; ++cb) {                                                           \
        const s16x8 bb = (cb & 2) ? b1 : b0;                                                                       \
        const s16x4 bf = (cb & 1) ? __builtin_shufflevector(bb, bb, 4, 5, 6, 7) : __builtin_shufflevector(bb, bb, 0, 1, 2, 3); \
        t[cb] = __builtin_amdgcn_mfma_f32_16x16x16bf16_1k(bf, ident, t[cb], 0, 0, 0);                              \
      }                                                                                                            \
      const s16x4 cv[4] = {vv[0], vv[1], vv[2], vv[3]};                                                            \
      const h16x4 cs = sm;                                                                                         \
      GATE(0)                                                                                                      \
      if (i + 1 < nt) {                                                                                            \
        if (i + 2 < nt) asm volatile("s_waitcnt vmcnt(4)" ::: "memory");                                           \
        else asm volatile("s_waitcnt vmcnt(0)" ::: "memory");                                                      \
        b0 = lds_ld<s16x8>(aB0 + SN * 4096); b1 = lds_ld<s16x8>(aB1 + SN * 4096); oh = lds_ld<s16x4>(aOH + RN * 512); \
        _Pragma("unroll") for (int cb = 0; cb < 4; ++cb) vv[cb] = lds_tr(aV[cb] + SN * 4096);                      \
        sm = __builtin_bit_cast(h16x4, lds_tr(aS + RN * 512));                                                     \
        if (i + 3 < nt) idx_read(((RT) + 3) & 7);                                                                  \
      }                                                                                                            \
      GATE(1) GATE(2)                                                                                              \
      if (i + 3 < nt) dma_go(SN);                                                                                  \
      GATE(3)                                                                                                      \
      if ((RT) == 4 && i == 4) {                                                                                   \
        const int er = min(n0 + min(nl, max(nb - 1, 0)), N - 1);                                                   \
        const char* s2 = (lane & 16) ? AUs + (size_t)(er * 256 + 128) : (const char*)x + ((size_t)er * 256 + slice * 128); \
        asm volatile("s_mov_b32 m0, %0\n\ts_nop 0\n\tglobal_load_lds_dword %1, off" ::"s"(lb + 4 * STG + w * (OHR + IDR) + OHR + 256), "v"(s2) : "memory"); \
      }                                                                                                            \
      if ((RT) == 3) { if (i == 3) expand(raw[2], 0); else if (i == 11) expand(raw[4], 0); }                       \
      if ((RT) == 7) { if (i == 7) expand(raw[3], 1); }                                                            \
    }
#pragma nounroll
    for (int i0 = 0; i0 < nt; i0 += 8) {
      TILE_STEP(0) TILE_STEP(1) TILE_STEP(2) TILE_STEP(3) TILE_STEP(4) TILE_STEP(5) TILE_STEP(6) TILE_STEP(7)
    }
#undef TILE_STEP
#undef GATE
  }
  CYC1; STAMP(5);
  if (nl < nb) {
    const size_t o = (size_t)(n0 + nl) * 128 + slice * 64 + q * 16;
    const u32x4 x0 = *(const u32x4*)(x + o), x1 = *(const u32x4*)(x + o + 8);
    const char* up = AUs + (size_t)(n0 + nl) * 256 + 128 + q * 32;
    const u32x4 u0 = *(const u32x4*)up, u1 = *(const u32x4*)(up + 16);
    u32x4 r0, r1;
#pragma unroll
    for (int cb = 0; cb < 4; ++cb) {
      const int ix = (cb & 1) * 2;
      const u32 ua = (cb & 2) ? u1[ix] : u0[ix], ub = (cb & 2) ? u1[ix + 1] : u0[ix + 1];
      const float uv[4] = {bf_lo(ua), bf_hi(ua), bf_lo(ub), bf_hi(ub)};
      float z[4];
#pragma unroll
      for (int i = 0; i < 4; ++i) {
        const float agg = Yn[cb][i] * __builtin_amdgcn_rcpf(Yd[cb][i] + epsv);
        const float zz = uv[i] + agg;
        z[i] = zz > 0.f ? zz : 0.f * zz;
      }
      const u32 xa = (cb & 2) ? x1[ix] : x0[ix], xb = (cb & 2) ? x1[ix + 1] : x0[ix + 1];
      const u32 ra = pk_bf16(bf_lo(xa) + z[0], bf_hi(xa) + z[1]), rb = pk_bf16(bf_lo(xb) + z[2], bf_hi(xb) + z[3]);
      if (cb & 2) { r1[ix] = ra; r1[ix + 1] = rb; } else { r0[ix] = ra; r0[ix + 1] = rb; }
    }
    *(u32x4*)(out + o) = r0;
    *(u32x4*)(out + o + 8) = r1;
  }
  STAMP(6);
  STAMP_OUT(blockIdx.x * 4 + w);
}

extern "C" void kernel_launch(void* const* d_in, const int* in_sizes, int n_in, void* d_out, int out_size, void* d_ws,
                              size_t ws_size, hipStream_t stream) {
  const int N = in_sizes[0] / 128, E = in_sizes[2];
#define BKV 1536
  int Bk = BKV;
  if (N > 12 * Bk) Bk = ((N + 11) / 12 + 15) / 16 * 16;
  const int CH = ((E + NCH - 1) / NCH + 3) & ~3;
  const int GW = 104;
  char* ws = (char*)d_ws;
  size_t o = 0;
  char* T = ws + o; o += (size_t)2 * N * 256; o = (o + 255) & ~(size_t)255;
  char* AU = ws + o; o += (size_t)2 * N * 256; o = (o + 255) & ~(size_t)255;
  u32* rec = (u32*)(ws + o); o += (size_t)NCH * CH * 12 + 512; o = (o + 255) & ~(size_t)255;
  u32* off = (u32*)(ws + o); o += (size_t)NCH * Bk * 4; o = (o + 255) & ~(size_t)255;
  u32* vmax = (u32*)(ws + o); o += (size_t)GW * 8 * 4; o = (o + 255) & ~(size_t)255;
  u16* wcp = (u16*)(ws + o);
  const u64 magN = ((1ull << 48) + (u64)N - 1) / (u64)N, magB = ((1ull << 48) + (u64)Bk - 1) / (u64)Bk;
  int lds1 = Bk * 2 + 64 + CH * 12 + 16;
  if (lds1 < 65536 + 16) lds1 = 65536 + 16;
  (void)hipFuncSetAttribute((const void*)k_prep, hipFuncAttributeMaxDynamicSharedMemorySize, lds1);
  k_prep<<<NCH + 2 * GW, K1T, lds1, stream>>>((const u16*)d_in[0], (const u16*)d_in[1], (const int*)d_in[2], (const int*)d_in[3],
                                             (const u16*)d_in[4], (const u16*)d_in[5], (const u16*)d_in[6], (const u16*)d_in[7],
                                             (const u16*)d_in[8], T, AU, rec, off, vmax, wcp, magN, magB, N, E, Bk, CH, GW);
  k_edge<<<Bk / 4 * 2, 256, K2LDS, stream>>>((const u16*)d_in[0], wcp, T, AU, rec, off, vmax, (u16*)d_out, magB, N, Bk, CH, GW);
}
```

```cpp
#include <hip/hip_runtime.h>
#include <stdint.h>

typedef unsigned int u32;
typedef unsigned short u16;
typedef unsigned long long u64;
using s16x4 = __attribute__((ext_vector_type(4))) short;
using s16x8 = __attribute__((ext_vector_type(8))) short;
using h16x4 = __attribute__((ext_vector_type(4))) _Float16;
using h16x2 = __attribute__((ext_vector_type(2))) _Float16;
using b16x2 = __attribute__((ext_vector_type(2))) __bf16;
using f32x4 = __attribute__((ext_vector_type(4))) float;
using f32x2 = __attribute__((ext_vector_type(2))) float;
using u32x2 = __attribute__((ext_vector_type(2))) u32;
using u32x3 = __attribute__((ext_vector_type(3))) u32;
using u32x4 = __attribute__((ext_vector_type(4))) u32;
#define LAS __attribute__((address_space(3)))
#define DI __device__ __forceinline__

constexpr int NCH = 64;
constexpr int EPT = 13;
constexpr int K1T = 512;
constexpr int GBLK = 7;
constexpr float NLOG2E = -1.4426950408889634f;
constexpr int NG = 5;
constexpr int WIN = NG * 64;

typedef LAS char* lptr;
template <class T> DI T lds_ld(lptr a) { return *(LAS T*)a; }
template <class T> DI void lds_st(lptr a, T v) { *(LAS T*)a = v; }
DI s16x4 lds_tr(lptr a) { return __builtin_amdgcn_ds_read_tr16_b64_v4i16((LAS s16x4*)a); }
DI u32 pk_bf16(float lo, float hi) { f32x2 v = {lo, hi}; return __builtin_bit_cast(u32, __builtin_convertvector(v, b16x2)); }
DI float bf_lo(u32 w) { return __uint_as_float(w << 16); }
DI float bf_hi(u32 w) { return __uint_as_float(w & 0xffff0000u); }

DI void dma_g(u32 m0v, const void* base, u32 voff) {
  asm volatile("s_mov_b32 m0, %0\n\ts_nop 0\n\tglobal_load_lds_dwordx4 %1, %2" ::"s"(m0v), "v"(voff), "s"(base) : "memory");
}
DI void dma_b(u32 m0v, u32x4 rsrc, u32x2 io) {
  asm volatile("s_mov_b32 m0, %0\n\ts_nop 0\n\tbuffer_load_dwordx4 %1, %2, 0 idxen offen lds" ::"s"(m0v), "v"(io), "s"(rsrc) : "memory");
}

#define LORD 1
#define STP 0
DI void gst4(void* p, u32x4 v) {
  *(u32x4*)p = v;
}
DI void gst4g(void* p, u32x4 v) {
  *(u32x4*)p = v;
}
template <int CTRL, int RM> DI u32 dpp0(u32 v) { return (u32)__builtin_amdgcn_update_dpp(0, (int)v, CTRL, RM, 0xf, false); }
DI u32 wave_scan_add(u32 v) {
  v += dpp0<0x111, 0xf>(v); v += dpp0<0x112, 0xf>(v); v += dpp0<0x114, 0xf>(v); v += dpp0<0x118, 0xf>(v);
  v += dpp0<0x142, 0xa>(v); v += dpp0<0x143, 0xc>(v);
  return v;
}
DI u32 umax(u32 a, u32 b) { return a > b ? a : b; }
DI u32 wave_scan_max(u32 v) {
  v = umax(v, dpp0<0x111, 0xf>(v)); v = umax(v, dpp0<0x112, 0xf>(v)); v = umax(v, dpp0<0x114, 0xf>(v)); v = umax(v, dpp0<0x118, 0xf>(v));
  v = umax(v, dpp0<0x142, 0xa>(v)); v = umax(v, dpp0<0x143, 0xc>(v));
  return v;
}
DI u32 divm(u32 x, u64 magic) { return (u32)(((u64)x * magic) >> 48); }

#define STAMP(k)
#define STAMP_DECL
#define HWID
#define CYC0
#define CYC1
#define STAMP_OUT(id)
__global__ __launch_bounds__(K1T) void k_prep(const u16* __restrict__ x, const u16* __restrict__ ea, const int* __restrict__ src,
                                             const int* __restrict__ dst, const u16* __restrict__ wa, const u16* __restrict__ wb,
                                             const u16* __restrict__ wc, const u16* __restrict__ wu, const u16* __restrict__ wv,
                                             char* __restrict__ T, char* __restrict__ AU, u32* __restrict__ rec,
                                             u32* __restrict__ off, u32* __restrict__ vmax, u16* __restrict__ wcp, u64 magN,
                                             u64 magB, int N, int E, int Bk, int CH, int GW) {
  extern __shared__ __attribute__((aligned(16))) char smem[];
  const lptr L = (lptr)smem;
  const u32 lb = (u32)(uintptr_t)L;
  const int tid = threadIdx.x, lane = tid & 63;
  const int w = __builtin_amdgcn_readfirstlane(tid >> 6);
  STAMP_DECL; STAMP(0);
  if ((int)blockIdx.x < NCH) {
    const int c = blockIdx.x;
    const int e0 = c * CH, e1 = min(E, e0 + CH);
    u32* hist = (u32*)smem;
    u32* wsum = hist + Bk / 2;
    u32* stage = wsum + 16;
    int dv[EPT], sv[EPT];
    u32x2 ev[EPT];
#pragma unroll
    for (int i = 0; i < EPT; ++i) { const int e = e0 + tid + i * K1T; dv[i] = dst[e < e1 ? e : 0]; }
#pragma unroll
    for (int i = 0; i < EPT; ++i) {
      const int e = e0 + tid + i * K1T, ee = e < e1 ? e : 0;
      sv[i] = src[ee]; ev[i] = *(const u32x2*)(ea + (size_t)ee * 4);
    }
    for (int i = tid; i < Bk / 2; i += K1T) hist[i] = 0;
    asm volatile("s_waitcnt lgkmcnt(0)" ::: "memory");
    __builtin_amdgcn_s_barrier();
    STAMP(1);
    u32 br[EPT];
#pragma unroll
    for (int i = 0; i < EPT; ++i) {
      const int e = e0 + tid + i * K1T;
      const u32 bk = divm((u32)(dv[i] + 1) * (u32)Bk - 1u, magN);
      const u32 sh = (bk & 1u) << 4;
      u32 r = 0;
      if (e < e1) r = atomicAdd(&hist[bk >> 1], 1u << sh);
      br[i] = bk | ((r >> sh) << 16);
    }
    asm volatile("" ::: "memory");
#pragma unroll
    for (int i = 0; i < EPT; ++i) {
      const int e = e0 + tid + i * K1T, ee = e < e1 ? e : 0;
    }
    asm volatile("s_waitcnt lgkmcnt(0)" ::: "memory");
    __builtin_amdgcn_s_barrier();
    STAMP(2);
    u32 carry = 0;
    for (int sb0 = 0; sb0 < Bk / 4; sb0 += K1T) {
      const int sbi = sb0 + tid;
      u32x2 cn = {0u, 0u};
      if (sbi < Bk / 4) cn = *(const u32x2*)(hist + sbi * 2);
      const u32 k0 = cn[0] & 0xffffu, k1 = cn[0] >> 16, k2 = cn[1] & 0xffffu, k3 = cn[1] >> 16;
      const u32 loc = k0 + k1 + k2 + k3;
      const u32 inc = wave_scan_add(loc);
      if (lane == 63) wsum[w] = inc;
      asm volatile("s_waitcnt lgkmcnt(0)" ::: "memory");
      __builtin_amdgcn_s_barrier();
      const u32x4 s0 = *(const u32x4*)wsum, s1 = *(const u32x4*)(wsum + 4);
      const u32 base = carry + inc - loc + (w > 0 ? s0[0] : 0u) + (w > 1 ? s0[1] : 0u) + (w > 2 ? s0[2] : 0u) + (w > 3 ? s0[3] : 0u) +
                       (w > 4 ? s1[0] : 0u) + (w > 5 ? s1[1] : 0u) + (w > 6 ? s1[2] : 0u);
      carry += s0[0] + s0[1] + s0[2] + s0[3] + s1[0] + s1[1] + s1[2] + s1[3];
      if (sbi < Bk / 4) {
        const u32 b1 = base + k0, b2 = b1 + k1, b3 = b2 + k2;
        *(u32x2*)(hist + sbi * 2) = u32x2{base | (b1 << 16), b2 | (b3 << 16)};
        *(u32x4*)(off + ((size_t)sbi * NCH + (c & 7) * (NCH / 8) + (c >> 3)) * 4) = u32x4{base | (k0 << 16), b1 | (k1 << 16), b2 | (k2 << 16), b3 | (k3 << 16)};
      }
      asm volatile("s_waitcnt lgkmcnt(0)" ::: "memory");
      __builtin_amdgcn_s_barrier();
    }
    STAMP(3);
#pragma unroll
    for (int i = 0; i < EPT; ++i) {
      const int e = e0 + tid + i * K1T;
      if (e < e1) {
        const u32 bk = br[i] & 0xffffu;
        const u32 pos = (u32)((const u16*)hist)[bk] + (br[i] >> 16);
        const u32 dl = (u32)dv[i] - divm(bk * (u32)N, magB);
        u32* d = stage + pos * 3;
        d[0] = ev[i][0]; d[1] = ev[i][1]; d[2] = (u32)sv[i] | (dl << 24);
      }
    }
    asm volatile("s_waitcnt lgkmcnt(0)" ::: "memory");
    __builtin_amdgcn_s_barrier();
    STAMP(4);
    {
      const int nq = ((e1 - e0) * 3 + 3) >> 2;
      u32x4* g = (u32x4*)(rec + (size_t)c * CH * 3);
      for (int i = tid; i < nq; i += K1T) gst4(g + i, *(const u32x4*)(stage + i * 4));
    }
    STAMP(5);
    STAMP_OUT(4096 + blockIdx.x * 8 + w);
    return;
  }
  const int g = blockIdx.x - NCH;
  const int mat = (g >> 3) & 3, gi = (g & 7) + 8 * (g >> 5);
  const int r0 = gi * 128;
  const int q = lane >> 4, qq = (lane >> 2) & 3, p = lane & 3, nl = lane & 15;
  const u32 XO = 32768, MO = 65536;
  if (g == 0) {
    const int s = tid >> 8, j = (tid >> 6) & 3, cb = (tid >> 4) & 3, n = tid & 15;
    const int lam = 16 * (n >> 2) + 4 * cb + (n & 3);
    const float v = __uint_as_float((u32)wc[j * 128 + 64 * s + lam] << 16) * NLOG2E;
    wcp[tid] = (u16)(pk_bf16(v, 0.f) & 0xffffu);
  }
  if (r0 >= N) { if (mat == 1 && tid == 0) vmax[gi] = 0u; return; }
  {
    const u16* W = mat == 0 ? wb : mat == 1 ? wv : mat == 2 ? wa : wu;
#pragma unroll
    for (int i = 0; i < 4; ++i) {
      const int m = w * 4 + i;
      const int k = m * 4 + q;
      dma_g(lb + XO + m * 1024, x, (u32)(min(r0 + k, N - 1) * 256 + ((nl ^ (k & 15)) << 4)));
    }
#pragma unroll
    for (int i = 0; i < 4; ++i) {
      const int m = w * 4 + i;
      const int k = m * 4 + q;
      const int fw = (k & 1) | ((k & 2) << 2);
      dma_g(lb + m * 1024, W, (u32)(k * 256 + ((nl ^ fw) << 4)));
    }
  }
  if (tid == 0) lds_st<u32>(L + MO, 0u);
  STAMP(1);
  asm volatile("s_waitcnt vmcnt(0) lgkmcnt(0)" ::: "memory");
  __builtin_amdgcn_s_barrier();
  STAMP(2);
  const int s = (w >> 1) & 1, kq = w & 1, hb = w >> 2;
  s16x8 wf[2][4];
#pragma unroll
  for (int be = 0; be < 2; ++be) {
    const int lamB = 16 * p + 8 * kq + 4 * be, lamU = 32 * kq + 8 * p + 4 * be, lamV = 16 * (2 * (p & 1) + be) + 4 * (2 * kq + (p >> 1));
    const int lam0 = mat == 0 ? lamB : mat == 3 ? lamU : lamV;
    const int byte = 2 * (64 * s + lam0);
#pragma unroll
    for (int ks = 0; ks < 4; ++ks) {
      s16x4 h[2];
#pragma unroll
      for (int hh = 0; hh < 2; ++hh) {
        const int k = 32 * ks + 8 * q + 4 * hh + qq;
        const int fw = (k & 1) | ((k & 2) << 2);
        h[hh] = lds_tr(L + (k * 256 + (((byte >> 4) ^ fw) << 4) + (byte & 8)));
      }
      wf[be][ks] = __builtin_shufflevector(h[0], h[1], 0, 1, 2, 3, 4, 5, 6, 7);
    }
  }
  u32 vm = 0;
  char* dbase = ((mat & 2) ? AU : T) + (size_t)s * N * 256 + (mat & 1) * 128 + (4 * kq + q) * 16;
  const float scl = (mat & 1) ? 1.f : NLOG2E;
#pragma unroll
  for (int bi = 0; bi < 4; ++bi) {
    const int lr = (hb * 4 + bi) * 16 + nl;
    f32x4 a0 = {0.f, 0.f, 0.f, 0.f}, a1 = {0.f, 0.f, 0.f, 0.f};
#pragma unroll
    for (int ks = 0; ks < 4; ++ks) {
      const s16x8 xf = lds_ld<s16x8>(L + (XO + lr * 256 + (((4 * ks + q) ^ nl) << 4)));
      a0 = __builtin_amdgcn_mfma_f32_16x16x32_bf16(wf[0][ks], xf, a0, 0, 0, 0);
      a1 = __builtin_amdgcn_mfma_f32_16x16x32_bf16(wf[1][ks], xf, a1, 0, 0, 0);
    }
    u32x4 o;
    if (mat == 1) {
      h16x2 h0 = {(_Float16)a0[0], (_Float16)a0[1]}, h1 = {(_Float16)a0[2], (_Float16)a0[3]};
      h16x2 h2 = {(_Float16)a1[0], (_Float16)a1[1]}, h3 = {(_Float16)a1[2], (_Float16)a1[3]};
      o[0] = __builtin_bit_cast(u32, h0); o[1] = __builtin_bit_cast(u32, h1);
      o[2] = __builtin_bit_cast(u32, h2); o[3] = __builtin_bit_cast(u32, h3);
#pragma unroll
      for (int i = 0; i < 4; ++i) { vm = umax(vm, __float_as_uint(a0[i]) & 0x7fffffffu); vm = umax(vm, __float_as_uint(a1[i]) & 0x7fffffffu); }
    } else {
      o[0] = pk_bf16(a0[0] * scl, a0[1] * scl); o[1] = pk_bf16(a0[2] * scl, a0[3] * scl);
      o[2] = pk_bf16(a1[0] * scl, a1[1] * scl); o[3] = pk_bf16(a1[2] * scl, a1[3] * scl);
    }
    if (r0 + lr < N) gst4g(dbase + (size_t)(r0 + lr) * 256, o);
  }
  STAMP(4);
  STAMP(5);
  STAMP_OUT(4096 + blockIdx.x * 8 + w);
  if (mat == 1) {
    const u32 m = wave_scan_max(vm);
    if (lane == 63) atomicMax((u32*)(smem + MO), m);
    asm volatile("s_waitcnt lgkmcnt(0)" ::: "memory");
    __builtin_amdgcn_s_barrier();
    if (tid == 0) vmax[gi] = lds_ld<u32>(L + MO);
  }
}

#define EXP_ID(x) (x)
constexpr u32 STG = 8192;
constexpr u32 OHR = 4096, IDR = 512;
constexpr u32 K2LDS = 4 * STG + 4 * (OHR + IDR) + 1024;

__global__ __launch_bounds__(256, 3) void k_edge(const u16* __restrict__ x, const u16* __restrict__ wcp, const char* __restrict__ T,
                                                const char* __restrict__ AU, const u32* __restrict__ rec,
                                                const u32* __restrict__ off, const u32* __restrict__ vmax,
                                                u16* __restrict__ out, u64 magB, int N, int Bk, int CH, int nvm) {
  extern __shared__ __attribute__((aligned(16))) char smem[];
  const lptr L = (lptr)smem;
  const u32 lb = (u32)(uintptr_t)L;
  const int tid = threadIdx.x, lane = tid & 63;
  const int w = __builtin_amdgcn_readfirstlane(tid >> 6);
  STAMP_DECL; STAMP(0);
  const int r8 = blockIdx.x & 7, slice = r8 & 1;
  const int sb = (blockIdx.x >> 3) * 4 + (r8 >> 1);
  const int b = sb * 4 + w;
  const int q = lane >> 4, nl = lane & 15, qq = (lane >> 2) & 3, p = lane & 3;
  const lptr stg = L + w * STG, ohr = L + 4 * STG + w * (OHR + IDR), idr = ohr + OHR;
  const u32 stgm0 = lb + w * STG;

  const int n0 = (int)divm((u32)b * (u32)N, magB), n1 = (int)divm((u32)(b + 1) * (u32)N, magB);
  const int nb = n1 - n0;
  const char* Ts = T + (size_t)slice * N * 256;
  const char* AUs = AU + (size_t)slice * N * 256;
  const int chunk = (lane & 7) * (NCH / 8) + (lane >> 3);
  const u32x4 o4 = *(const u32x4*)(off + ((size_t)sb * NCH + lane) * 4);
  const u32 cid = (u32)((lane % (NCH / 8)) * 8 + lane / (NCH / 8));
  (void)chunk;
  {
    const int rr = lane >> 3, pos = lane & 7;
    const int r0 = min(n0 + min(rr, max(nb - 1, 0)), N - 1), r1 = min(n0 + min(8 + rr, max(nb - 1, 0)), N - 1);
    const u32 awm0 = lb + 4 * STG + w * (OHR + IDR) + 2048;
    dma_g(awm0, AUs, (u32)(r0 * 256 + pos * 16));
    const char* s1 = rr < 4 ? AUs + (size_t)(r1 * 256 + pos * 16) : (const char*)wcp + (slice * 512 + (rr - 4) * 128 + pos * 16);
    asm volatile("s_mov_b32 m0, %0\n\ts_nop 0\n\tglobal_load_lds_dwordx4 %1, off" ::"s"(awm0 + 1024), "v"(s1) : "memory");
  }
  u32 vmx = 0;
  for (int i = lane; i < nvm; i += 64) vmx = umax(vmx, vmax[i]);

  const u32 swz = (u32)(nl & 7) << 1;
  lptr aB0 = stg + (nl * 256 + ((q ^ swz) << 4)), aB1 = stg + (nl * 256 + (((4 + q) ^ swz) << 4));
  lptr aV[4];
  {
    const u32 row = 4 * q + qq, sw = (row & 7) << 1;
#pragma unroll
    for (int cb = 0; cb < 4; ++cb) aV[cb] = stg + (row * 256 + (((8 + 2 * cb + (p >> 1)) ^ sw) << 4) + (p & 1) * 8);
  }
  lptr aOH = ohr + (nl * 32 + q * 8), aS = ohr + lane * 8, aI = idr + q * 2;
  u32 voff[4];
  voff[0] = voff[2] = ((u32)nl ^ ((u32)q << 1)) << 4; voff[1] = voff[3] = ((u32)nl ^ ((u32)(4 + q) << 1)) << 4;
  u32x4 rsrc;
  {
    const u64 tbp = (u64)(uintptr_t)Ts;
    rsrc[0] = (u32)tbp; rsrc[1] = ((u32)(tbp >> 32) & 0xffffu) | (256u << 16); rsrc[2] = (u32)N; rsrc[3] = 0x00020000u;
  }
  s16x4 ident;
#pragma unroll
  for (int j = 0; j < 4; ++j) ident[j] = (4 * q + j == nl) ? (short)0x3f80 : (short)0;
  f32x4 Yd[4], Yn[4];
#pragma unroll
  for (int cb = 0; cb < 4; ++cb) { Yd[cb] = f32x4{0.f, 0.f, 0.f, 0.f}; Yn[cb] = f32x4{0.f, 0.f, 0.f, 0.f}; }
  asm volatile("" : "+v"(aB0), "+v"(aB1), "+v"(aV[0]), "+v"(aV[1]), "+v"(aV[2]), "+v"(aV[3]), "+v"(aOH), "+v"(aS), "+v"(aI));
  asm volatile("" : "+v"(voff[0]), "+v"(voff[1]), "+v"(voff[2]), "+v"(voff[3]), "+v"(ident));
  asm volatile("" : "+v"(Yd[0]), "+v"(Yd[1]), "+v"(Yd[2]), "+v"(Yd[3]), "+v"(Yn[0]), "+v"(Yn[1]), "+v"(Yn[2]), "+v"(Yn[3]));

  const u32 gst = cid * (u32)CH + (o4[0] & 0xffffu);
  const u32 rlen = (o4[3] & 0xffffu) + (o4[3] >> 16) - (o4[0] & 0xffffu);
  {
    const u32 l31 = (u32)lane & 31u;
#pragma unroll
    for (int m = 0; m < 8; ++m) {
      const u32 ga = __builtin_amdgcn_readlane(gst, w * 16 + 2 * m), gb = __builtin_amdgcn_readlane(gst, w * 16 + 2 * m + 1);
      const u32 la = __builtin_amdgcn_readlane(rlen, w * 16 + 2 * m), lb2 = __builtin_amdgcn_readlane(rlen, w * 16 + 2 * m + 1);
      const u32 vo = ((lane < 32 ? ga : gb) + l31) * 12u;
      if (l31 < (lane < 32 ? la : lb2))
        asm volatile("s_mov_b32 m0, %0\n\ts_nop 0\n\tglobal_load_lds_dwordx3 %1, %2" ::"s"(lb + (w * 16 + 2 * m) * 512), "v"(vo), "s"(rec) : "memory");
    }
  }
  STAMP(1);
  vmx = __builtin_amdgcn_readlane(wave_scan_max(vmx), 63);
  int ea_ = 15 - ((int)(vmx >> 23) - 126);
  ea_ = ea_ > 15 ? 15 : ea_;
  const float cneg = (float)(-ea_), cadd = __uint_as_float((u32)(127 - ea_) << 23);
  const float epsv = 1e-6f * 1.875f * __uint_as_float((u32)(127 + ea_) << 23);
  const f32x2 cadd2 = {cadd, cadd};
  const f32x4 cinit = {0.f, 0.f, 0.f, 0.f};
  u32 o0 = w == 0 ? o4[0] : w == 1 ? o4[1] : w == 2 ? o4[2] : o4[3];
  u32x3 raw[NG];
  const u32x3 padrec = {0u, 0u, 15u << 24};
  const u32 inc0 = wave_scan_add(o0 >> 16);
  const int ntot = (int)__builtin_amdgcn_readlane(inc0, 63);
  const bool fast = ntot <= WIN;
  u32 gd0 = cid * (u32)CH + (o0 & 0xffffu) - (inc0 - (o0 >> 16));
  auto setup = [&](int w0) {
    const int nw = min(ntot - w0, WIN);
    const u32 c0 = o0 >> 16;
    const u32 pre0 = wave_scan_add(c0) - c0;
    const lptr mk = ohr, dt = ohr + 512;
    lds_st<u32x2>(mk + lane * 8, u32x2{0u, 0u});
    lds_st<u32>(dt + lane * 4, gd0);
    __builtin_amdgcn_wave_barrier();
    if (c0 && (int)(pre0 + c0) > w0 && (int)pre0 < w0 + WIN) lds_st<unsigned char>(mk + max((int)pre0 - w0, 0), (unsigned char)(lane + 1));
    __builtin_amdgcn_wave_barrier();
    u32 carry = 0;
#pragma unroll
    for (int g = 0; g < NG; ++g) {
      raw[g] = padrec;
      if (g * 64 < nw) {
        u32 id = lds_ld<unsigned char>(mk + lane + g * 64);
        id = umax(wave_scan_max(id), carry);
        carry = __builtin_amdgcn_readlane(id, 63);
        const int j = g * 64 + lane;
        const u32 d = lds_ld<u32>(dt + (id - 1) * 4);
        if (j < nw) raw[g] = *(const u32x3*)(rec + (size_t)(u32)(w0 + j + d) * 3);
      }
    }
    __builtin_amdgcn_wave_barrier();
  };
  u32x2 en[NG];
  if (fast) {
    const u32 c0 = o0 >> 16, pre0 = inc0 - c0;
    const u32 ra = (o0 & 0xffffu) - (o4[0] & 0xffffu);
    const lptr mk = ohr, dt = ohr + 512;
    lds_st<u32x2>(mk + lane * 8, u32x2{0u, 0u});
    const int lim = max(min((int)pre0 + 32 - (int)ra, 0x7fff), 0);
    lds_st<u32x2>(dt + lane * 8, u32x2{(((u32)lane * 32u + ra - pre0) & 0xffffu) | ((u32)lim << 16), gd0});
    __builtin_amdgcn_wave_barrier();
    if (c0) lds_st<unsigned char>(mk + pre0, (unsigned char)(lane + 1));
    __builtin_amdgcn_wave_barrier();
    u32 carry = 0;
#pragma unroll
    for (int g = 0; g < NG; ++g) {
      if (g * 64 < ntot) {
        u32 id = lds_ld<unsigned char>(mk + lane + g * 64);
        id = umax(wave_scan_max(id), carry);
        carry = __builtin_amdgcn_readlane(id, 63);
        en[g] = lds_ld<u32x2>(dt + (id - 1) * 8);
      }
    }
  }
  asm volatile("s_waitcnt vmcnt(0) lgkmcnt(0)" ::: "memory");
  __builtin_amdgcn_s_barrier();
  STAMP(2);
  s16x4 AW[4];
#pragma unroll
  for (int cb = 0; cb < 4; ++cb) {
    AW[cb] = lds_tr(ohr + (2048 + (4 * q + qq) * 128 + cb * 32 + p * 8));
    if (q == 2) AW[cb][3] = (short)(__float_as_uint(cneg) >> 16);
  }
  if (fast) {
#pragma unroll
    for (int g = 0; g < NG; ++g) {
      raw[g] = padrec;
      if (g * 64 < ntot) {
        const int j = g * 64 + lane;
        const bool ovf = j >= (int)(en[g][0] >> 16);
        if (j < ntot) {
          if (!ovf) raw[g] = lds_ld<u32x3>(L + ((j + (int)(short)en[g][0]) << 4));
          else raw[g] = *(const u32x3*)(rec + (size_t)(u32)(j + en[g][1]) * 3);
        }
      }
    }
  }
  asm volatile("s_waitcnt lgkmcnt(0)" ::: "memory");
  __builtin_amdgcn_s_barrier();
  auto expand = [&](const u32x3& r, int half) {
    const lptr a = ohr + lane * 32 + half * 2048;
    lds_st<u32x4>(a, u32x4{0u, 0u, 0u, 0u});
    lds_st<u32x4>(a + 16, u32x4{0u, 0x3f800000u, r[0], r[1]});
    const u32 dl = (r[2] >> 24) & 15u;
    if (dl < 11) lds_st<u16>(a + dl * 2, (u16)0x3f80);
    lds_st<u16>(idr + lane * 2 + half * 128, (u16)r[2]);
  };
  HWID;
  STAMP(4); CYC0;
#pragma nounroll
  for (int w0 = 0; w0 < ntot; w0 += WIN) {
    if (w0 || !fast) { asm volatile("" : "+v"(o0), "+v"(gd0)); setup(w0); }
    const int nw = min(ntot - w0, WIN);
    asm volatile("" : "+v"(raw[0]), "+v"(raw[1]), "+v"(raw[2]), "+v"(raw[3]), "+v"(raw[4]));
    expand(raw[0], 0);
    expand(raw[1], 1);
    const int nt = (nw + 31) >> 5 << 1;
    u32 id[4];
    auto idx_read = [&](int rt) {
#pragma unroll
      for (int k = 0; k < 4; ++k) id[k] = lds_ld<u16>(aI + rt * 32 + k * 8);
    };
    auto dma_go = [&](int slot) {
      asm volatile("s_waitcnt lgkmcnt(0)" ::: "memory");
#pragma unroll
      for (int k = 0; k < 4; ++k) dma_b(stgm0 + slot * 4096 + k * 1024, rsrc, u32x2{id[k], voff[k]});
    };
    s16x8 b0, b1;
    s16x4 oh, vv[4];
    h16x4 sm;
    idx_read(0); dma_go(0);
    idx_read(1); dma_go(1);
    asm volatile("s_waitcnt vmcnt(4)" ::: "memory");
    b0 = lds_ld<s16x8>(aB0); b1 = lds_ld<s16x8>(aB1); oh = lds_ld<s16x4>(aOH);
#pragma unroll
    for (int cb = 0; cb < 4; ++cb) vv[cb] = lds_tr(aV[cb]);
    sm = __builtin_bit_cast(h16x4, lds_tr(aS));
    idx_read(2); dma_go(0);
#define SB
#define GATE(cb)                                                                                                   \
      {                                                                                                            \
        f32x2 d0 = {__builtin_amdgcn_exp2f(t[cb][0]), __builtin_amdgcn_exp2f(t[cb][1])};                           \
        f32x2 d1 = {__builtin_amdgcn_exp2f(t[cb][2]), __builtin_amdgcn_exp2f(t[cb][3])};                           \
        d0 += cadd2; d1 += cadd2;                                                                                  \
        const h16x2 e0 = __builtin_bit_cast(h16x2, __builtin_amdgcn_cvt_pkrtz(__builtin_amdgcn_rcpf(d0[0]), __builtin_amdgcn_rcpf(d0[1]))); \
        const h16x2 e1 = __builtin_bit_cast(h16x2, __builtin_amdgcn_cvt_pkrtz(__builtin_amdgcn_rcpf(d1[0]), __builtin_amdgcn_rcpf(d1[1]))); \
        const h16x4 e = __builtin_shufflevector(e0, e1, 0, 1, 2, 3);                                               \
        const h16x4 pr = e * __builtin_bit_cast(h16x4, cv[cb]);                                                    \
        Yd[cb] = __builtin_amdgcn_mfma_f32_16x16x16f16(e, cs, Yd[cb], 0, 0, 0);                                    \
        Yn[cb] = __builtin_amdgcn_mfma_f32_16x16x16f16(pr, cs, Yn[cb], 0, 0, 0);                                   \
      }
#define TILE_STEP(RT)                                                                                              \
    {                                                                                                              \
      constexpr int SN = ((RT) + 1) & 1, RN = ((RT) + 1) & 7;                                                      \
      f32x4 t[4];                                                                                                  \
      _Pragma("unroll") for (int cb = 0; cb < 4; ++cb)                                                             \
        t[cb] = __builtin_amdgcn_mfma_f32_16x16x16bf16_1k(oh, AW[cb], cinit, 0, 0, 0);                             \
      _Pragma("unroll") for (int cb = 0; cb < 4; ++cb) {                                                           \
        const s16x8 bb = (cb & 2) ? b1 : b0;                                                                       \
        const s16x4 bf = (cb & 1) ? __builtin_shufflevector(bb, bb, 4, 5, 6, 7) : __builtin_shufflevector(bb, bb, 0, 1, 2, 3); \
        t[cb] = __builtin_amdgcn_mfma_f32_16x16x16bf16_1k(bf, ident, t[cb], 0, 0, 0);                              \
      }                                                                                                            \
      const s16x4 cv[4] = {vv[0], vv[1], vv[2], vv[3]};                                                            \
      const h16x4 cs = sm;                                                                                         \
      GATE(0)                                                                                                      \
      SB asm volatile("s_waitcnt vmcnt(4)" ::: "memory");                                                          \
      b0 = lds_ld<s16x8>(aB0 + SN * 4096); b1 = lds_ld<s16x8>(aB1 + SN * 4096); oh = lds_ld<s16x4>(aOH + RN * 512); \
      _Pragma("unroll") for (int cb = 0; cb < 4; ++cb) vv[cb] = lds_tr(aV[cb] + SN * 4096);                        \
      sm = __builtin_bit_cast(h16x4, lds_tr(aS + RN * 512));                                                       \
      idx_read(((RT) + 3) & 7);                                                                                    \
      SB GATE(1) GATE(2)                                                                                           \
      SB dma_go(SN);                                                                                               \
      SB GATE(3) SB                                                                                                \
    }
#pragma nounroll
    for (int i0 = 0; i0 < nt; i0 += 8) {
      TILE_STEP(0) TILE_STEP(1)
      if (i0 + 2 < nt) {
        TILE_STEP(2) TILE_STEP(3)
        if (i0 == 0) {
          expand(raw[2], 0);
          const int er = min(n0 + min(nl, max(nb - 1, 0)), N - 1);
          const char* s2 = (lane & 16) ? AUs + (size_t)(er * 256 + 128) : (const char*)x + ((size_t)er * 256 + slice * 128);
          asm volatile("s_mov_b32 m0, %0\n\ts_nop 0\n\tglobal_load_lds_dword %1, off" ::"s"(lb + 4 * STG + w * (OHR + IDR) + OHR + 256), "v"(s2) : "memory");
        } else if (i0 == 8) expand(raw[4], 0);
        if (i0 + 4 < nt) {
          TILE_STEP(4) TILE_STEP(5)
          if (i0 + 6 < nt) {
            TILE_STEP(6) TILE_STEP(7)
            if (i0 == 0) expand(raw[3], 1);
          }
        }
      }
    }
    asm volatile("s_waitcnt vmcnt(0) lgkmcnt(0)" ::: "memory");
#undef TILE_STEP
#undef GATE
  }
  CYC1; STAMP(5);
  if (nl < nb) {
    const size_t o = (size_t)(n0 + nl) * 128 + slice * 64 + q * 16;
    const u32x4 x0 = *(const u32x4*)(x + o), x1 = *(const u32x4*)(x + o + 8);
    const char* up = AUs + (size_t)(n0 + nl) * 256 + 128 + q * 32;
    const u32x4 u0 = *(const u32x4*)up, u1 = *(const u32x4*)(up + 16);
    u32x4 r0, r1;
#pragma unroll
    for (int cb = 0; cb < 4; ++cb) {
      const int ix = (cb & 1) * 2;
      const u32 ua = (cb & 2) ? u1[ix] : u0[ix], ub = (cb & 2) ? u1[ix + 1] : u0[ix + 1];
      const float uv[4] = {bf_lo(ua), bf_hi(ua), bf_lo(ub), bf_hi(ub)};
      float z[4];
#pragma unroll
      for (int i = 0; i < 4; ++i) {
        const float agg = Yn[cb][i] * __builtin_amdgcn_rcpf(Yd[cb][i] + epsv);
        const float zz = uv[i] + agg;
        z[i] = zz > 0.f ? zz : 0.f * zz;
      }
      const u32 xa = (cb & 2) ? x1[ix] : x0[ix], xb = (cb & 2) ? x1[ix + 1] : x0[ix + 1];
      const u32 ra = pk_bf16(bf_lo(xa) + z[0], bf_hi(xa) + z[1]), rb = pk_bf16(bf_lo(xb) + z[2], bf_hi(xb) + z[3]);
      if (cb & 2) { r1[ix] = ra; r1[ix + 1] = rb; } else { r0[ix] = ra; r0[ix + 1] = rb; }
    }
    *(u32x4*)(out + o) = r0;
    *(u32x4*)(out + o + 8) = r1;
  }
  STAMP(6);
  STAMP_OUT(blockIdx.x * 4 + w);
}

extern "C" void kernel_launch(void* const* d_in, const int* in_sizes, int n_in, void* d_out, int out_size, void* d_ws,
                              size_t ws_size, hipStream_t stream) {
  const int N = in_sizes[0] / 128, E = in_sizes[2];
#define BKV 1536
  int Bk = BKV;
  if (N > 10 * Bk) Bk = ((N + 9) / 10 + 15) / 16 * 16;
  const int CH = ((E + NCH - 1) / NCH + 3) & ~3;
  const int GW = ((N + 127) / 128 + 7) / 8 * 8;
  char* ws = (char*)d_ws;
  size_t o = 0;
  char* T = ws + o; o += (size_t)2 * N * 256; o = (o + 255) & ~(size_t)255;
  char* AU = ws + o; o += (size_t)2 * N * 256; o = (o + 255) & ~(size_t)255;
  u32* rec = (u32*)(ws + o); o += (size_t)NCH * CH * 12 + 512; o = (o + 255) & ~(size_t)255;
  u32* off = (u32*)(ws + o); o += (size_t)NCH * Bk * 4; o = (o + 255) & ~(size_t)255;
  u32* vmax = (u32*)(ws + o); o += (size_t)GW * 8 * 4; o = (o + 255) & ~(size_t)255;
  u16* wcp = (u16*)(ws + o);
  const u64 magN = ((1ull << 48) + (u64)N - 1) / (u64)N, magB = ((1ull << 48) + (u64)Bk - 1) / (u64)Bk;
  int lds1 = Bk * 2 + 64 + CH * 12 + 16;
  if (lds1 < 65536 + 16) lds1 = 65536 + 16;
  (void)hipFuncSetAttribute((const void*)k_prep, hipFuncAttributeMaxDynamicSharedMemorySize, lds1);
  k_prep<<<NCH + 4 * GW, K1T, lds1, stream>>>((const u16*)d_in[0], (const u16*)d_in[1], (const int*)d_in[2], (const int*)d_in[3],
                                             (const u16*)d_in[4], (const u16*)d_in[5], (const u16*)d_in[6], (const u16*)d_in[7],
                                             (const u16*)d_in[8], T, AU, rec, off, vmax, wcp, magN, magB, N, E, Bk, CH, GW);
  k_edge<<<Bk / 4 * 2, 256, K2LDS, stream>>>((const u16*)d_in[0], wcp, T, AU, rec, off, vmax, (u16*)d_out, magB, N, Bk, CH, GW);
}
```

```cpp
#include <hip/hip_runtime.h>
#include <stdint.h>

typedef unsigned int u32;
typedef unsigned short u16;
typedef unsigned long long u64;
using s16x4 = __attribute__((ext_vector_type(4))) short;
using s16x8 = __attribute__((ext_vector_type(8))) short;
using h16x4 = __attribute__((ext_vector_type(4))) _Float16;
using h16x2 = __attribute__((ext_vector_type(2))) _Float16;
using b16x2 = __attribute__((ext_vector_type(2))) __bf16;
using f32x4 = __attribute__((ext_vector_type(4))) float;
using f32x2 = __attribute__((ext_vector_type(2))) float;
using u32x2 = __attribute__((ext_vector_type(2))) u32;
using u32x3 = __attribute__((ext_vector_type(3))) u32;
using u32x4 = __attribute__((ext_vector_type(4))) u32;
#define LAS __attribute__((address_space(3)))
#define DI __device__ __forceinline__

constexpr int NCH = 64;
constexpr int EPT = 13;
constexpr int K1T = 512;
constexpr int GBLK = 7;
constexpr float NLOG2E = -1.4426950408889634f;
constexpr int NG = 6;
constexpr int WIN = NG * 64;

typedef LAS char* lptr;
template <class T> DI T lds_ld(lptr a) { return *(LAS T*)a; }
template <class T> DI void lds_st(lptr a, T v) { *(LAS T*)a = v; }
DI s16x4 lds_tr(lptr a) { return __builtin_amdgcn_ds_read_tr16_b64_v4i16((LAS s16x4*)a); }
DI u32 pk_bf16(float lo, float hi) { f32x2 v = {lo, hi}; return __builtin_bit_cast(u32, __builtin_convertvector(v, b16x2)); }
DI float bf_lo(u32 w) { return __uint_as_float(w << 16); }
DI float bf_hi(u32 w) { return __uint_as_float(w & 0xffff0000u); }

DI void dma_g(u32 m0v, const void* base, u32 voff) {
  asm volatile("s_mov_b32 m0, %0\n\ts_nop 0\n\tglobal_load_lds_dwordx4 %1, %2" ::"s"(m0v), "v"(voff), "s"(base) : "memory");
}
DI void dma_b(u32 m0v, u32x4 rsrc, u32x2 io) {
  asm volatile("s_mov_b32 m0, %0\n\ts_nop 0\n\tbuffer_load_dwordx4 %1, %2, 0 idxen offen lds" ::"s"(m0v), "v"(io), "s"(rsrc) : "memory");
}

#define LORD 1
#define STP 0
DI void gst4(void* p, u32x4 v) {
  *(u32x4*)p = v;
}
DI void gst4g(void* p, u32x4 v) {
  *(u32x4*)p = v;
}
template <int CTRL, int RM> DI u32 dpp0(u32 v) { return (u32)__builtin_amdgcn_update_dpp(0, (int)v, CTRL, RM, 0xf, false); }
DI u32 wave_scan_add(u32 v) {
  v += dpp0<0x111, 0xf>(v); v += dpp0<0x112, 0xf>(v); v += dpp0<0x114, 0xf>(v); v += dpp0<0x118, 0xf>(v);
  v += dpp0<0x142, 0xa>(v); v += dpp0<0x143, 0xc>(v);
  return v;
}
DI u32 umax(u32 a, u32 b) { return a > b ? a : b; }
DI u32 wave_scan_max(u32 v) {
  v = umax(v, dpp0<0x111, 0xf>(v)); v = umax(v, dpp0<0x112, 0xf>(v)); v = umax(v, dpp0<0x114, 0xf>(v)); v = umax(v, dpp0<0x118, 0xf>(v));
  v = umax(v, dpp0<0x142, 0xa>(v)); v = umax(v, dpp0<0x143, 0xc>(v));
  return v;
}
DI int fresh_lane() {
  int l = (int)__builtin_amdgcn_mbcnt_hi(~0u, __builtin_amdgcn_mbcnt_lo(~0u, 0u));
  asm volatile("" : "+v"(l));
  return l;
}
DI u32 divm(u32 x, u64 magic) { return (u32)(((u64)x * magic) >> 48); }

#define STAMP(k)
#define STAMP_DECL
#define HWID
#define CYC0
#define CYC1
#define STAMP_OUT(id)
__global__ __launch_bounds__(K1T) void k_prep(const u16* __restrict__ x, const u16* __restrict__ ea, const int* __restrict__ src,
                                             const int* __restrict__ dst, const u16* __restrict__ wa, const u16* __restrict__ wb,
                                             const u16* __restrict__ wc, const u16* __restrict__ wu, const u16* __restrict__ wv,
                                             char* __restrict__ T, char* __restrict__ AU, u32* __restrict__ rec,
                                             u32* __restrict__ off, u32* __restrict__ vmax, u16* __restrict__ wcp, u64 magN,
                                             u64 magB, int N, int E, int Bk, int CH, int GW) {
  extern __shared__ __attribute__((aligned(16))) char smem[];
  const lptr L = (lptr)smem;
  const u32 lb = (u32)(uintptr_t)L;
  const int tid = threadIdx.x, lane = tid & 63;
  const int w = __builtin_amdgcn_readfirstlane(tid >> 6);
  STAMP_DECL; STAMP(0);
  if ((int)blockIdx.x < NCH) {
    const int c = blockIdx.x;
    const int e0 = c * CH, e1 = min(E, e0 + CH);
    u32* hist = (u32*)smem;
    u32* wsum = hist + Bk / 2;
    u32* stage = wsum + 16;
    int dv[EPT], sv[EPT];
    u32x2 ev[EPT];
#pragma unroll
    for (int i = 0; i < EPT; ++i) { const int e = e0 + tid + i * K1T; dv[i] = dst[e < e1 ? e : 0]; }
#pragma unroll
    for (int i = 0; i < EPT; ++i) {
      const int e = e0 + tid + i * K1T, ee = e < e1 ? e : 0;
      sv[i] = src[ee]; ev[i] = *(const u32x2*)(ea + (size_t)ee * 4);
    }
    for (int i = tid; i < Bk / 2; i += K1T) hist[i] = 0;
    asm volatile("s_waitcnt lgkmcnt(0)" ::: "memory");
    __builtin_amdgcn_s_barrier();
    STAMP(1);
    u32 br[EPT];
#pragma unroll
    for (int i = 0; i < EPT; ++i) {
      const int e = e0 + tid + i * K1T;
      const u32 bk = divm((u32)(dv[i] + 1) * (u32)Bk - 1u, magN);
      const u32 sh = (bk & 1u) << 4;
      u32 r = 0;
      if (e < e1) r = atomicAdd(&hist[bk >> 1], 1u << sh);
      br[i] = bk | ((r >> sh) << 16);
    }
    asm volatile("" ::: "memory");
#pragma unroll
    for (int i = 0; i < EPT; ++i) {
      const int e = e0 + tid + i * K1T, ee = e < e1 ? e : 0;
    }
    asm volatile("s_waitcnt lgkmcnt(0)" ::: "memory");
    __builtin_amdgcn_s_barrier();
    STAMP(2);
    u32 carry = 0;
    for (int sb0 = 0; sb0 < Bk / 4; sb0 += K1T) {
      const int sbi = sb0 + tid;
      u32x2 cn = {0u, 0u};
      if (sbi < Bk / 4) cn = *(const u32x2*)(hist + sbi * 2);
      const u32 k0 = cn[0] & 0xffffu, k1 = cn[0] >> 16, k2 = cn[1] & 0xffffu, k3 = cn[1] >> 16;
      const u32 loc = k0 + k1 + k2 + k3;
      const u32 inc = wave_scan_add(loc);
      if (lane == 63) wsum[w] = inc;
      asm volatile("s_waitcnt lgkmcnt(0)" ::: "memory");
      __builtin_amdgcn_s_barrier();
      const u32x4 s0 = *(const u32x4*)wsum, s1 = *(const u32x4*)(wsum + 4);
      const u32 base = carry + inc - loc + (w > 0 ? s0[0] : 0u) + (w > 1 ? s0[1] : 0u) + (w > 2 ? s0[2] : 0u) + (w > 3 ? s0[3] : 0u) +
                       (w > 4 ? s1[0] : 0u) + (w > 5 ? s1[1] : 0u) + (w > 6 ? s1[2] : 0u);
      carry += s0[0] + s0[1] + s0[2] + s0[3] + s1[0] + s1[1] + s1[2] + s1[3];
      if (sbi < Bk / 4) {
        const u32 b1 = base + k0, b2 = b1 + k1, b3 = b2 + k2;
        *(u32x2*)(hist + sbi * 2) = u32x2{base | (b1 << 16), b2 | (b3 << 16)};
        *(u32x4*)(off + ((size_t)sbi * NCH + (c & 7) * (NCH / 8) + (c >> 3)) * 4) = u32x4{base | (k0 << 16), b1 | (k1 << 16), b2 | (k2 << 16), b3 | (k3 << 16)};
      }
      asm volatile("s_waitcnt lgkmcnt(0)" ::: "memory");
      __builtin_amdgcn_s_barrier();
    }
    STAMP(3);
#pragma unroll
    for (int i = 0; i < EPT; ++i) {
      const int e = e0 + tid + i * K1T;
      if (e < e1) {
        const u32 bk = br[i] & 0xffffu;
        const u32 pos = (u32)((const u16*)hist)[bk] + (br[i] >> 16);
        const u32 dl = (u32)dv[i] - divm(bk * (u32)N, magB);
        u32* d = stage + pos * 3;
        d[0] = ev[i][0]; d[1] = ev[i][1]; d[2] = (u32)sv[i] | (dl << 24);
      }
    }
    asm volatile("s_waitcnt lgkmcnt(0)" ::: "memory");
    __builtin_amdgcn_s_barrier();
    STAMP(4);
    {
      const int nq = ((e1 - e0) * 3 + 3) >> 2;
      u32x4* g = (u32x4*)(rec + (size_t)c * CH * 3);
      for (int i = tid; i < nq; i += K1T) gst4(g + i, *(const u32x4*)(stage + i * 4));
    }
    STAMP(5);
    STAMP_OUT(4096 + blockIdx.x * 8 + w);
    return;
  }
  const int g = blockIdx.x - NCH;
  const int mat = (g >> 3) & 3, gi = (g & 7) + 8 * (g >> 5);
  const int r0 = gi * 128;
  const int q = lane >> 4, qq = (lane >> 2) & 3, p = lane & 3, nl = lane & 15;
  const u32 XO = 32768, MO = 65536;
  if (g == 0) {
    const int s = tid >> 8, j = (tid >> 6) & 3, cb = (tid >> 4) & 3, n = tid & 15;
    const int lam = 16 * (n >> 2) + 4 * cb + (n & 3);
    const float v = __uint_as_float((u32)wc[j * 128 + 64 * s + lam] << 16) * NLOG2E;
    wcp[tid] = (u16)(pk_bf16(v, 0.f) & 0xffffu);
  }
  if (r0 >= N) { if (mat == 1 && tid == 0) vmax[gi] = 0u; return; }
  {
    const u16* W = mat == 0 ? wb : mat == 1 ? wv : mat == 2 ? wa : wu;
#pragma unroll
    for (int i = 0; i < 4; ++i) {
      const int m = w * 4 + i;
      const int k = m * 4 + q;
      dma_g(lb + XO + m * 1024, x, (u32)(min(r0 + k, N - 1) * 256 + ((nl ^ (k & 15)) << 4)));
    }
#pragma unroll
    for (int i = 0; i < 4; ++i) {
      const int m = w * 4 + i;
      const int k = m * 4 + q;
      const int fw = (k & 1) | ((k & 2) << 2);
      dma_g(lb + m * 1024, W, (u32)(k * 256 + ((nl ^ fw) << 4)));
    }
  }
  if (tid == 0) lds_st<u32>(L + MO, 0u);
  STAMP(1);
  asm volatile("s_waitcnt vmcnt(0) lgkmcnt(0)" ::: "memory");
  __builtin_amdgcn_s_barrier();
  STAMP(2);
  const int s = (w >> 1) & 1, kq = w & 1, hb = w >> 2;
  s16x8 wf[2][4];
#pragma unroll
  for (int be = 0; be < 2; ++be) {
    const int lamB = 16 * p + 8 * kq + 4 * be, lamU = 32 * kq + 8 * p + 4 * be, lamV = 16 * (2 * (p & 1) + be) + 4 * (2 * kq + (p >> 1));
    const int lam0 = mat == 0 ? lamB : mat == 3 ? lamU : lamV;
    const int byte = 2 * (64 * s + lam0);
#pragma unroll
    for (int ks = 0; ks < 4; ++ks) {
      s16x4 h[2];
#pragma unroll
      for (int hh = 0; hh < 2; ++hh) {
        const int k = 32 * ks + 8 * q + 4 * hh + qq;
        const int fw = (k & 1) | ((k & 2) << 2);
        h[hh] = lds_tr(L + (k * 256 + (((byte >> 4) ^ fw) << 4) + (byte & 8)));
      }
      wf[be][ks] = __builtin_shufflevector(h[0], h[1], 0, 1, 2, 3, 4, 5, 6, 7);
    }
  }
  u32 vm = 0;
  char* dbase = ((mat & 2) ? AU : T) + (size_t)s * N * 256 + (mat & 1) * 128 + (4 * kq + q) * 16;
  const float scl = (mat & 1) ? 1.f : NLOG2E;
#pragma unroll
  for (int bi = 0; bi < 4; ++bi) {
    const int lr = (hb * 4 + bi) * 16 + nl;
    f32x4 a0 = {0.f, 0.f, 0.f, 0.f}, a1 = {0.f, 0.f, 0.f, 0.f};
#pragma unroll
    for (int ks = 0; ks < 4; ++ks) {
      const s16x8 xf = lds_ld<s16x8>(L + (XO + lr * 256 + (((4 * ks + q) ^ nl) << 4)));
      a0 = __builtin_amdgcn_mfma_f32_16x16x32_bf16(wf[0][ks], xf, a0, 0, 0, 0);
      a1 = __builtin_amdgcn_mfma_f32_16x16x32_bf16(wf[1][ks], xf, a1, 0, 0, 0);
    }
    u32x4 o;
    if (mat == 1) {
      h16x2 h0 = {(_Float16)a0[0], (_Float16)a0[1]}, h1 = {(_Float16)a0[2], (_Float16)a0[3]};
      h16x2 h2 = {(_Float16)a1[0], (_Float16)a1[1]}, h3 = {(_Float16)a1[2], (_Float16)a1[3]};
      o[0] = __builtin_bit_cast(u32, h0); o[1] = __builtin_bit_cast(u32, h1);
      o[2] = __builtin_bit_cast(u32, h2); o[3] = __builtin_bit_cast(u32, h3);
#pragma unroll
      for (int i = 0; i < 4; ++i) { vm = umax(vm, __float_as_uint(a0[i]) & 0x7fffffffu); vm = umax(vm, __float_as_uint(a1[i]) & 0x7fffffffu); }
    } else {
      o[0] = pk_bf16(a0[0] * scl, a0[1] * scl); o[1] = pk_bf16(a0[2] * scl, a0[3] * scl);
      o[2] = pk_bf16(a1[0] * scl, a1[1] * scl); o[3] = pk_bf16(a1[2] * scl, a1[3] * scl);
    }
    if (r0 + lr < N) gst4g(dbase + (size_t)(r0 + lr) * 256, o);
  }
  STAMP(4);
  STAMP(5);
  STAMP_OUT(4096 + blockIdx.x * 8 + w);
  if (mat == 1) {
    const u32 m = wave_scan_max(vm);
    if (lane == 63) atomicMax((u32*)(smem + MO), m);
    asm volatile("s_waitcnt lgkmcnt(0)" ::: "memory");
    __builtin_amdgcn_s_barrier();
    if (tid == 0) vmax[gi] = lds_ld<u32>(L + MO);
  }
}

#define EXP_ID(x) (x)
constexpr u32 STG = 8192;
constexpr u32 OHR = 4096, IDR = 512;
constexpr u32 K2LDS = 4 * STG + 4 * (OHR + IDR) + 1024;

__global__ __launch_bounds__(256, 3) void k_edge(const u16* __restrict__ x, const u16* __restrict__ wv, const u16* __restrict__ wcp, const char* __restrict__ T,
                                                const char* __restrict__ AU, const u32* __restrict__ rec,
                                                const u32* __restrict__ off, const u32* __restrict__ vmax,
                                                u16* __restrict__ out, u64 magB, int N, int Bk, int CH, int nvm) {
  extern __shared__ __attribute__((aligned(16))) char smem[];
  const lptr L = (lptr)smem;
  const u32 lb = (u32)(uintptr_t)L;
  const int tid = threadIdx.x, lane = tid & 63;
  const int w = __builtin_amdgcn_readfirstlane(tid >> 6);
  STAMP_DECL; STAMP(0);
  const int r8 = blockIdx.x & 7, slice = r8 & 1;
  const int sb = (blockIdx.x >> 3) * 4 + (r8 >> 1);
  const int b = sb * 4 + w;
  const int q = lane >> 4, nl = lane & 15, qq = (lane >> 2) & 3, p = lane & 3;
  const lptr stg = L + w * STG, ohr = L + 4 * STG + w * (OHR + IDR), idr = ohr + OHR;
  const u32 stgm0 = lb + w * STG;

  const int n0 = (int)divm((u32)b * (u32)N, magB), n1 = (int)divm((u32)(b + 1) * (u32)N, magB);
  const int nb = n1 - n0;
  const char* Ts = T + (size_t)slice * N * 256;
  const char* AUs = AU + (size_t)slice * N * 256;
  const int chunk = (lane & 7) * (NCH / 8) + (lane >> 3);
  const u32x4 o4 = *(const u32x4*)(off + ((size_t)sb * NCH + lane) * 4);
  const u32 cid = (u32)((lane % (NCH / 8)) * 8 + lane / (NCH / 8));
  (void)chunk;
  {
    const int rr = lane >> 3, pos = lane & 7;
    const int r0 = min(n0 + min(rr, max(nb - 1, 0)), N - 1), r1 = min(n0 + min(8 + rr, max(nb - 1, 0)), N - 1);
    const u32 awm0 = lb + 4 * STG + w * (OHR + IDR) + 2048;
    dma_g(awm0, AUs, (u32)(r0 * 256 + pos * 16));
    const char* s1 = rr < 4 ? AUs + (size_t)(r1 * 256 + pos * 16) : (const char*)wcp + (slice * 512 + (rr - 4) * 128 + pos * 16);
    asm volatile("s_mov_b32 m0, %0\n\ts_nop 0\n\tglobal_load_lds_dwordx4 %1, off" ::"s"(awm0 + 1024), "v"(s1) : "memory");
  }
  u32 vmx = 0;
  for (int i = lane; i < nvm; i += 64) vmx = umax(vmx, vmax[i]);

  const u32 swz = (u32)(nl & 7) << 1;
  lptr aB0 = stg + (nl * 256 + ((q ^ swz) << 4)), aB1 = stg + (nl * 256 + (((4 + q) ^ swz) << 4));
  lptr aV[4];
  {
    const u32 row = 4 * q + qq, sw = (row & 7) << 1;
#pragma unroll
    for (int cb = 0; cb < 4; ++cb) aV[cb] = stg + (row * 256 + (((8 + 2 * cb + (p >> 1)) ^ sw) << 4) + (p & 1) * 8);
  }
  lptr aOH = ohr + (nl * 32 + q * 8), aS = ohr + lane * 8, aI = idr + q * 2;
  u32 voff[4];
  voff[0] = voff[2] = ((u32)nl ^ ((u32)q << 1)) << 4; voff[1] = voff[3] = ((u32)nl ^ ((u32)(4 + q) << 1)) << 4;
  u32x4 rsrc;
  {
    const u64 tbp = (u64)(uintptr_t)Ts;
    rsrc[0] = (u32)tbp; rsrc[1] = ((u32)(tbp >> 32) & 0xffffu) | (256u << 16); rsrc[2] = (u32)N; rsrc[3] = 0x00020000u;
  }
  s16x4 ident;
#pragma unroll
  for (int j = 0; j < 4; ++j) ident[j] = (4 * q + j == nl) ? (short)0x3f80 : (short)0;
  f32x4 Yd[4], Yn[4];
#pragma unroll
  for (int cb = 0; cb < 4; ++cb) { Yd[cb] = f32x4{0.f, 0.f, 0.f, 0.f}; Yn[cb] = f32x4{0.f, 0.f, 0.f, 0.f}; }
  asm volatile("" : "+v"(aB0), "+v"(aB1), "+v"(aV[0]), "+v"(aV[1]), "+v"(aV[2]), "+v"(aV[3]), "+v"(aOH), "+v"(aS), "+v"(aI));
  asm volatile("" : "+v"(voff[0]), "+v"(voff[1]), "+v"(voff[2]), "+v"(voff[3]), "+v"(ident));

  const u32 gst = cid * (u32)CH + (o4[0] & 0xffffu);
  const u32 rlen = (o4[3] & 0xffffu) + (o4[3] >> 16) - (o4[0] & 0xffffu);
  {
    const u32 l31 = (u32)lane & 31u;
#pragma unroll
    for (int m = 0; m < 8; ++m) {
      const u32 ga = __builtin_amdgcn_readlane(gst, w * 16 + 2 * m), gb = __builtin_amdgcn_readlane(gst, w * 16 + 2 * m + 1);
      const u32 la = __builtin_amdgcn_readlane(rlen, w * 16 + 2 * m), lb2 = __builtin_amdgcn_readlane(rlen, w * 16 + 2 * m + 1);
      const u32 vo = ((lane < 32 ? ga : gb) + l31) * 12u;
      if (l31 < (lane < 32 ? la : lb2))
        asm volatile("s_mov_b32 m0, %0\n\ts_nop 0\n\tglobal_load_lds_dwordx3 %1, %2" ::"s"(lb + (w * 16 + 2 * m) * 512), "v"(vo), "s"(rec) : "memory");
    }
  }
  STAMP(1);
  vmx = __builtin_amdgcn_readlane(wave_scan_max(vmx), 63);
  if (vmx != 0u && (vmx < 0x38800000u || vmx > 0x43000000u)) {
    asm volatile("s_waitcnt vmcnt(0) lgkmcnt(0)" ::: "memory");
    __builtin_amdgcn_s_barrier();
    const int lf = fresh_lane();
    const u32 ow = off[((size_t)sb * NCH + lf) * 4 + w];
    const lptr acc = stg + lf * 4;
    for (int n = 0; n < 24; ++n) lds_st<float>(acc + n * 256, 0.f);
    const int posB = ((lf >> 4) + 4 * ((lf >> 3) & 1)) * 16 + (lf & 7) * 2;
    const int pi = 16 * ((lf >> 2) & 3) + 4 * (lf >> 4) + (lf & 3);
    float wcf[4];
#pragma unroll
    for (int j = 0; j < 4; ++j) wcf[j] = bf_lo(wcp[slice * 256 + j * 64 + pi]);
    for (int t = 0; t < NCH; ++t) {
      const u32 ot = __builtin_amdgcn_readlane(ow, t);
      const u32 base = (u32)((t % (NCH / 8)) * 8 + t / (NCH / 8)) * (u32)CH + (ot & 0xffffu);
      for (u32 r = 0; r < (ot >> 16); ++r) {
        const u32* rp = rec + (size_t)(base + r) * 3;
        const u32 e01 = rp[0], e23 = rp[1], wd = rp[2];
        const u32 sn = wd & 0xffffffu, dl = (wd >> 24) & 15u;
        float tv = bf_lo(*(const u16*)(AUs + (size_t)(n0 + dl) * 256 + 2 * pi)) + bf_lo(*(const u16*)(Ts + (size_t)sn * 256 + posB));
        tv += bf_lo(e01) * wcf[0] + bf_hi(e01) * wcf[1] + bf_lo(e23) * wcf[2] + bf_hi(e23) * wcf[3];
        const float eta = 1.f / (1.f + __builtin_amdgcn_exp2f(tv));
        float v = 0.f;
        for (int k = 0; k < 128; ++k) v += bf_lo(x[(size_t)sn * 128 + k]) * bf_lo(wv[k * 128 + slice * 64 + lf]);
        lds_st<float>(acc + dl * 512, lds_ld<float>(acc + dl * 512) + eta);
        lds_st<float>(acc + dl * 512 + 256, lds_ld<float>(acc + dl * 512 + 256) + eta * v);
      }
    }
    for (int n = 0; n < nb; ++n) {
      const size_t o = (size_t)(n0 + n) * 128 + slice * 64 + lf;
      const float z = bf_lo(*(const u16*)(AUs + (size_t)(n0 + n) * 256 + 128 + lf * 2)) +
                      lds_ld<float>(acc + n * 512 + 256) / (lds_ld<float>(acc + n * 512) + 1e-6f);
      out[o] = (u16)(pk_bf16(bf_lo(x[o]) + (z > 0.f ? z : 0.f * z), 0.f) & 0xffffu);
    }
    return;
  }
  int ea_ = 15 - ((int)(vmx >> 23) - 126);
  ea_ = ea_ > 15 ? 15 : ea_;
  const float cneg = (float)(-ea_), cadd = __uint_as_float((u32)(127 - ea_) << 23);
  const float epsv = 1e-6f * 1.875f * __uint_as_float((u32)(127 + ea_) << 23);
  const f32x2 cadd2 = {cadd, cadd};
  const f32x4 cinit = {0.f, 0.f, 0.f, 0.f};
  const u32 o0 = w == 0 ? o4[0] : w == 1 ? o4[1] : w == 2 ? o4[2] : o4[3];
  u32x3 raw[NG];
  const u32x3 padrec = {0u, 0u, 15u << 24};
  const u32 inc0 = wave_scan_add(o0 >> 16);
  const int ntot = (int)__builtin_amdgcn_readlane(inc0, 63);
  const bool fast = ntot <= WIN;
  const u32 gd0 = cid * (u32)CH + (o0 & 0xffffu) - (inc0 - (o0 >> 16));
  auto setup = [&](int w0) {
    const int ln = fresh_lane();
    const int nw = min(ntot - w0, WIN);
    const u32 oo = off[((size_t)sb * NCH + ln) * 4 + w];
    const u32 c0 = oo >> 16;
    const u32 pre0 = wave_scan_add(c0) - c0;
    const u32 gd = (u32)((ln % (NCH / 8)) * 8 + ln / (NCH / 8)) * (u32)CH + (oo & 0xffffu) - pre0;
    const lptr mk = ohr, dt = ohr + 512;
    lds_st<u32x2>(mk + ln * 8, u32x2{0u, 0u});
    lds_st<u32>(dt + ln * 4, gd);
    __builtin_amdgcn_wave_barrier();
    if (c0 && (int)(pre0 + c0) > w0 && (int)pre0 < w0 + WIN) lds_st<unsigned char>(mk + max((int)pre0 - w0, 0), (unsigned char)(ln + 1));
    __builtin_amdgcn_wave_barrier();
    u32 carry = 0;
#pragma unroll
    for (int g = 0; g < NG; ++g) {
      raw[g] = padrec;
      if (g * 64 < nw) {
        u32 id = lds_ld<unsigned char>(mk + ln + g * 64);
        id = umax(wave_scan_max(id), carry);
        carry = __builtin_amdgcn_readlane(id, 63);
        const int j = g * 64 + ln;
        const u32 d = lds_ld<u32>(dt + (id - 1) * 4);
        if (j < nw) raw[g] = *(const u32x3*)(rec + (size_t)(u32)(w0 + j + d) * 3);
      }
    }
    __builtin_amdgcn_wave_barrier();
  };
  u32x2 en[NG];
  if (fast) {
    const u32 c0 = o0 >> 16, pre0 = inc0 - c0;
    const u32 ra = (o0 & 0xffffu) - (o4[0] & 0xffffu);
    const lptr mk = ohr, dt = ohr + 512;
    lds_st<u32x2>(mk + lane * 8, u32x2{0u, 0u});
    const int lim = max(min((int)pre0 + 32 - (int)ra, 0x7fff), 0);
    lds_st<u32x2>(dt + lane * 8, u32x2{(((u32)lane * 32u + ra - pre0) & 0xffffu) | ((u32)lim << 16), gd0});
    __builtin_amdgcn_wave_barrier();
    if (c0) lds_st<unsigned char>(mk + pre0, (unsigned char)(lane + 1));
    __builtin_amdgcn_wave_barrier();
    u32 carry = 0;
#pragma unroll
    for (int g = 0; g < NG; ++g) {
      if (g * 64 < ntot) {
        u32 id = lds_ld<unsigned char>(mk + lane + g * 64);
        id = umax(wave_scan_max(id), carry);
        carry = __builtin_amdgcn_readlane(id, 63);
        en[g] = lds_ld<u32x2>(dt + (id - 1) * 8);
      }
    }
  }
  asm volatile("s_waitcnt vmcnt(0) lgkmcnt(0)" ::: "memory");
  __builtin_amdgcn_s_barrier();
  STAMP(2);
  s16x4 AW[4];
#pragma unroll
  for (int cb = 0; cb < 4; ++cb) {
    AW[cb] = lds_tr(ohr + (2048 + (4 * q + qq) * 128 + cb * 32 + p * 8));
    if (q == 2) AW[cb][3] = (short)(__float_as_uint(cneg) >> 16);
  }
  if (fast) {
#pragma unroll
    for (int g = 0; g < NG; ++g) {
      raw[g] = padrec;
      if (g * 64 < ntot) {
        const int j = g * 64 + lane;
        const bool ovf = j >= (int)(en[g][0] >> 16);
        if (j < ntot) {
          if (!ovf) raw[g] = lds_ld<u32x3>(L + ((j + (int)(short)en[g][0]) << 4));
          else raw[g] = *(const u32x3*)(rec + (size_t)(u32)(j + en[g][1]) * 3);
        }
      }
    }
  }
  asm volatile("s_waitcnt lgkmcnt(0)" ::: "memory");
  __builtin_amdgcn_s_barrier();
  auto expand = [&](const u32x3& r, int half) {
    const lptr a = ohr + lane * 32 + half * 2048;
    lds_st<u32x4>(a, u32x4{0u, 0u, 0u, 0u});
    lds_st<u32x4>(a + 16, u32x4{0u, 0x3f800000u, r[0], r[1]});
    const u32 dl = (r[2] >> 24) & 15u;
    if (dl < 11) lds_st<u16>(a + dl * 2, (u16)0x3f80);
    lds_st<u16>(idr + lane * 2 + half * 128, (u16)r[2]);
  };
  HWID;
  STAMP(4); CYC0;
#pragma nounroll
  for (int w0 = 0; w0 < ntot; w0 += WIN) {
    if (w0 || !fast) setup(w0);
    const int nw = min(ntot - w0, WIN);
    asm volatile("" : "+v"(raw[0]), "+v"(raw[1]), "+v"(raw[2]), "+v"(raw[3]), "+v"(raw[4]), "+v"(raw[5]));
    expand(raw[0], 0);
    expand(raw[1], 1);
    const int nt = (nw + 31) >> 5 << 1;
    u32 id[4];
    auto idx_read = [&](int rt) {
#pragma unroll
      for (int k = 0; k < 4; ++k) id[k] = lds_ld<u16>(aI + rt * 32 + k * 8);
    };
    auto dma_go = [&](int slot) {
      asm volatile("s_waitcnt lgkmcnt(0)" ::: "memory");
#pragma unroll
      for (int k = 0; k < 4; ++k) dma_b(stgm0 + slot * 4096 + k * 1024, rsrc, u32x2{id[k], voff[k]});
    };
    s16x8 b0, b1;
    s16x4 oh, vv[4];
    h16x4 sm;
    idx_read(0); dma_go(0);
    idx_read(1); dma_go(1);
    asm volatile("s_waitcnt vmcnt(4)" ::: "memory");
    b0 = lds_ld<s16x8>(aB0); b1 = lds_ld<s16x8>(aB1); oh = lds_ld<s16x4>(aOH);
#pragma unroll
    for (int cb = 0; cb < 4; ++cb) vv[cb] = lds_tr(aV[cb]);
    sm = __builtin_bit_cast(h16x4, lds_tr(aS));
    idx_read(2); dma_go(0);
#define SB
#define GATE(cb)                                                                                                   \
      {                                                                                                            \
        f32x2 d0 = {__builtin_amdgcn_exp2f(t[cb][0]), __builtin_amdgcn_exp2f(t[cb][1])};                           \
        f32x2 d1 = {__builtin_amdgcn_exp2f(t[cb][2]), __builtin_amdgcn_exp2f(t[cb][3])};                           \
        d0 += cadd2; d1 += cadd2;                                                                                  \
        const h16x2 e0 = __builtin_bit_cast(h16x2, __builtin_amdgcn_cvt_pkrtz(__builtin_amdgcn_rcpf(d0[0]), __builtin_amdgcn_rcpf(d0[1]))); \
        const h16x2 e1 = __builtin_bit_cast(h16x2, __builtin_amdgcn_cvt_pkrtz(__builtin_amdgcn_rcpf(d1[0]), __builtin_amdgcn_rcpf(d1[1]))); \
        const h16x4 e = __builtin_shufflevector(e0, e1, 0, 1, 2, 3);                                               \
        const h16x4 pr = e * __builtin_bit_cast(h16x4, cv[cb]);                                                    \
        Yd[cb] = __builtin_amdgcn_mfma_f32_16x16x16f16(e, cs, Yd[cb], 0, 0, 0);                                    \
        Yn[cb] = __builtin_amdgcn_mfma_f32_16x16x16f16(pr, cs, Yn[cb], 0, 0, 0);                                   \
      }
#define TILE_STEP(RT)                                                                                              \
    {                                                                                                              \
      constexpr int SN = ((RT) + 1) & 1, RN = ((RT) + 1) & 7;                                                      \
      f32x4 t[4];                                                                                                  \
      _Pragma("unroll") for (int cb = 0; cb < 4; ++cb)                                                             \
        t[cb] = __builtin_amdgcn_mfma_f32_16x16x16bf16_1k(oh, AW[cb], cinit, 0, 0, 0);                             \
      _Pragma("unroll") for (int cb = 0; cb < 4; ++cb) {                                                           \
        const s16x8 bb = (cb & 2) ? b1 : b0;                                                                       \
        const s16x4 bf = (cb & 1) ? __builtin_shufflevector(bb, bb, 4, 5, 6, 7) : __builtin_shufflevector(bb, bb, 0, 1, 2, 3); \
        t[cb] = __builtin_amdgcn_mfma_f32_16x16x16bf16_1k(bf, ident, t[cb], 0, 0, 0);                              \
      }                                                                                                            \
      const s16x4 cv[4] = {vv[0], vv[1], vv[2], vv[3]};                                                            \
      const h16x4 cs = sm;                                                                                         \
      GATE(0)                                                                                                      \
      SB asm volatile("s_waitcnt vmcnt(4)" ::: "memory");                                                          \
      b0 = lds_ld<s16x8>(aB0 + SN * 4096); b1 = lds_ld<s16x8>(aB1 + SN * 4096); oh = lds_ld<s16x4>(aOH + RN * 512); \
      _Pragma("unroll") for (int cb = 0; cb < 4; ++cb) vv[cb] = lds_tr(aV[cb] + SN * 4096);                        \
      sm = __builtin_bit_cast(h16x4, lds_tr(aS + RN * 512));                                                       \
      idx_read(((RT) + 3) & 7);                                                                                    \
      SB GATE(1) GATE(2)                                                                                           \
      SB dma_go(SN);                                                                                               \
      SB GATE(3) SB                                                                                                \
    }
#pragma nounroll
    for (int i0 = 0; i0 < nt; i0 += 8) {
      TILE_STEP(0) TILE_STEP(1)
      if (i0 + 2 < nt) {
        TILE_STEP(2) TILE_STEP(3)
        if (i0 == 0) {
          expand(raw[2], 0);
          const int lp = fresh_lane();
          const int er = min(n0 + min(lp & 15, max(nb - 1, 0)), N - 1);
          const char* s2 = (lp & 16) ? AUs + (size_t)(er * 256 + 128) : (const char*)x + ((size_t)er * 256 + slice * 128);
          asm volatile("s_mov_b32 m0, %0\n\ts_nop 0\n\tglobal_load_lds_dword %1, off" ::"s"(lb + 4 * STG + w * (OHR + IDR) + OHR + 256), "v"(s2) : "memory");
        } else if (i0 == 8) expand(raw[4], 0);
        if (i0 + 4 < nt) {
          TILE_STEP(4) TILE_STEP(5)
          if (i0 + 6 < nt) {
            TILE_STEP(6) TILE_STEP(7)
            if (i0 == 0) expand(raw[3], 1); else if (i0 == 8) expand(raw[5], 1);
          }
        }
      }
    }
    asm volatile("s_waitcnt vmcnt(0) lgkmcnt(0)" ::: "memory");
#undef TILE_STEP
#undef GATE
  }
  CYC1; STAMP(5);
  const int le = fresh_lane(), ne = le & 15, qe = le >> 4;
  if (ne < nb) {
    const size_t o = (size_t)(n0 + ne) * 128 + slice * 64 + qe * 16;
    const u32x4 x0 = *(const u32x4*)(x + o), x1 = *(const u32x4*)(x + o + 8);
    const char* up = AUs + (size_t)(n0 + ne) * 256 + 128 + qe * 32;
    const u32x4 u0 = *(const u32x4*)up, u1 = *(const u32x4*)(up + 16);
    u32x4 r0, r1;
#pragma unroll
    for (int cb = 0; cb < 4; ++cb) {
      const int ix = (cb & 1) * 2;
      const u32 ua = (cb & 2) ? u1[ix] : u0[ix], ub = (cb & 2) ? u1[ix + 1] : u0[ix + 1];
      const float uv[4] = {bf_lo(ua), bf_hi(ua), bf_lo(ub), bf_hi(ub)};
      float z[4];
#pragma unroll
      for (int i = 0; i < 4; ++i) {
        const float agg = Yn[cb][i] * __builtin_amdgcn_rcpf(Yd[cb][i] + epsv);
        const float zz = uv[i] + agg;
        z[i] = zz > 0.f ? zz : 0.f * zz;
      }
      const u32 xa = (cb & 2) ? x1[ix] : x0[ix], xb = (cb & 2) ? x1[ix + 1] : x0[ix + 1];
      const u32 ra = pk_bf16(bf_lo(xa) + z[0], bf_hi(xa) + z[1]), rb = pk_bf16(bf_lo(xb) + z[2], bf_hi(xb) + z[3]);
      if (cb & 2) { r1[ix] = ra; r1[ix + 1] = rb; } else { r0[ix] = ra; r0[ix + 1] = rb; }
    }
    *(u32x4*)(out + o) = r0;
    *(u32x4*)(out + o + 8) = r1;
  }
  STAMP(6);
  STAMP_OUT(blockIdx.x * 4 + w);
}

extern "C" void kernel_launch(void* const* d_in, const int* in_sizes, int n_in, void* d_out, int out_size, void* d_ws,
                              size_t ws_size, hipStream_t stream) {
  const int N = in_sizes[0] / 128, E = in_sizes[2];
#define BKV 1536
  int Bk = BKV;
  if (N > 10 * Bk) Bk = ((N + 9) / 10 + 15) / 16 * 16;
  const int CH = ((E + NCH - 1) / NCH + 3) & ~3;
  const int GW = ((N + 127) / 128 + 7) / 8 * 8;
  char* ws = (char*)d_ws;
  size_t o = 0;
  char* T = ws + o; o += (size_t)2 * N * 256; o = (o + 255) & ~(size_t)255;
  char* AU = ws + o; o += (size_t)2 * N * 256; o = (o + 255) & ~(size_t)255;
  u32* rec = (u32*)(ws + o); o += (size_t)NCH * CH * 12 + 512; o = (o + 255) & ~(size_t)255;
  u32* off = (u32*)(ws + o); o += (size_t)NCH * Bk * 4; o = (o + 255) & ~(size_t)255;
  u32* vmax = (u32*)(ws + o); o += (size_t)GW * 8 * 4; o = (o + 255) & ~(size_t)255;
  u16* wcp = (u16*)(ws + o);
  const u64 magN = ((1ull << 48) + (u64)N - 1) / (u64)N, magB = ((1ull << 48) + (u64)Bk - 1) / (u64)Bk;
  int lds1 = Bk * 2 + 64 + CH * 12 + 16;
  if (lds1 < 65536 + 16) lds1 = 65536 + 16;
  (void)hipFuncSetAttribute((const void*)k_prep, hipFuncAttributeMaxDynamicSharedMemorySize, lds1);
  k_prep<<<NCH + 4 * GW, K1T, lds1, stream>>>((const u16*)d_in[0], (const u16*)d_in[1], (const int*)d_in[2], (const int*)d_in[3],
                                             (const u16*)d_in[4], (const u16*)d_in[5], (const u16*)d_in[6], (const u16*)d_in[7],
                                             (const u16*)d_in[8], T, AU, rec, off, vmax, wcp, magN, magB, N, E, Bk, CH, GW);
  k_edge<<<Bk / 4 * 2, 256, K2LDS, stream>>>((const u16*)d_in[0], (const u16*)d_in[8], wcp, T, AU, rec, off, vmax, (u16*)d_out, magB, N, Bk, CH, GW);
}
```

```cpp
#include <hip/hip_runtime.h>
#include <stdint.h>

typedef unsigned int u32;
typedef unsigned short u16;
typedef unsigned long long u64;
using s16x4 = __attribute__((ext_vector_type(4))) short;
using s16x8 = __attribute__((ext_vector_type(8))) short;
using h16x4 = __attribute__((ext_vector_type(4))) _Float16;
using h16x2 = __attribute__((ext_vector_type(2))) _Float16;
using b16x2 = __attribute__((ext_vector_type(2))) __bf16;
using f32x4 = __attribute__((ext_vector_type(4))) float;
using f32x2 = __attribute__((ext_vector_type(2))) float;
using u32x2 = __attribute__((ext_vector_type(2))) u32;
using u32x3 = __attribute__((ext_vector_type(3))) u32;
using u32x4 = __attribute__((ext_vector_type(4))) u32;
#define LAS __attribute__((address_space(3)))
#define DI __device__ __forceinline__

constexpr int NCH = 64;
constexpr int EPT = 13;
constexpr int K1T = 512;
constexpr float NLOG2E = -1.4426950408889634f;
constexpr int NG = 6;
constexpr int WIN = NG * 64;

typedef LAS char* lptr;
template <class T> DI T lds_ld(lptr a) { return *(LAS T*)a; }
template <class T> DI void lds_st(lptr a, T v) { *(LAS T*)a = v; }
DI s16x4 lds_tr(lptr a) { return __builtin_amdgcn_ds_read_tr16_b64_v4i16((LAS s16x4*)a); }
DI u32 pk_bf16(float lo, float hi) { f32x2 v = {lo, hi}; return __builtin_bit_cast(u32, __builtin_convertvector(v, b16x2)); }
DI float bf_lo(u32 w) { return __uint_as_float(w << 16); }
DI float bf_hi(u32 w) { return __uint_as_float(w & 0xffff0000u); }

DI void dma_g(u32 m0v, const void* base, u32 voff) {
  asm volatile("s_mov_b32 m0, %0\n\ts_nop 0\n\tglobal_load_lds_dwordx4 %1, %2" ::"s"(m0v), "v"(voff), "s"(base) : "memory");
}
DI void dma_b(u32 m0v, u32x4 rsrc, u32x2 io) {
  asm volatile("s_mov_b32 m0, %0\n\ts_nop 0\n\tbuffer_load_dwordx4 %1, %2, 0 idxen offen lds" ::"s"(m0v), "v"(io), "s"(rsrc) : "memory");
}

template <int CTRL, int RM> DI u32 dpp0(u32 v) { return (u32)__builtin_amdgcn_update_dpp(0, (int)v, CTRL, RM, 0xf, false); }
DI u32 wave_scan_add(u32 v) {
  v += dpp0<0x111, 0xf>(v); v += dpp0<0x112, 0xf>(v); v += dpp0<0x114, 0xf>(v); v += dpp0<0x118, 0xf>(v);
  v += dpp0<0x142, 0xa>(v); v += dpp0<0x143, 0xc>(v);
  return v;
}
DI u32 umax(u32 a, u32 b) { return a > b ? a : b; }
DI u32 wave_scan_max(u32 v) {
  v = umax(v, dpp0<0x111, 0xf>(v)); v = umax(v, dpp0<0x112, 0xf>(v)); v = umax(v, dpp0<0x114, 0xf>(v)); v = umax(v, dpp0<0x118, 0xf>(v));
  v = umax(v, dpp0<0x142, 0xa>(v)); v = umax(v, dpp0<0x143, 0xc>(v));
  return v;
}
DI int fresh_lane() {
  int l = (int)__builtin_amdgcn_mbcnt_hi(~0u, __builtin_amdgcn_mbcnt_lo(~0u, 0u));
  asm volatile("" : "+v"(l));
  return l;
}
DI u32 divm(u32 x, u64 magic) { return (u32)(((u64)x * magic) >> 48); }

__global__ __launch_bounds__(K1T) void k_prep(const u16* __restrict__ x, const u16* __restrict__ ea, const int* __restrict__ src,
                                             const int* __restrict__ dst, const u16* __restrict__ wa, const u16* __restrict__ wb,
                                             const u16* __restrict__ wc, const u16* __restrict__ wu, const u16* __restrict__ wv,
                                             char* __restrict__ T, char* __restrict__ AU, u32* __restrict__ rec,
                                             u32* __restrict__ off, u32* __restrict__ vmax, u16* __restrict__ wcp, u64 magN,
                                             u64 magB, int N, int E, int Bk, int CH, int GW) {
  extern __shared__ __attribute__((aligned(16))) char smem[];
  const lptr L = (lptr)smem;
  const u32 lb = (u32)(uintptr_t)L;
  const int tid = threadIdx.x, lane = tid & 63;
  const int w = __builtin_amdgcn_readfirstlane(tid >> 6);
  if ((int)blockIdx.x < NCH) {
    const int c = blockIdx.x;
    const int e0 = c * CH, e1 = min(E, e0 + CH);
    u32* hist = (u32*)smem;
    u32* wsum = hist + Bk / 2;
    u32* stage = wsum + 16;
    int dv[EPT], sv[EPT];
    u32x2 ev[EPT];
#pragma unroll
    for (int i = 0; i < EPT; ++i) { const int e = e0 + tid + i * K1T; dv[i] = dst[e < e1 ? e : 0]; }
#pragma unroll
    for (int i = 0; i < EPT; ++i) {
      const int e = e0 + tid + i * K1T, ee = e < e1 ? e : 0;
      sv[i] = src[ee]; ev[i] = *(const u32x2*)(ea + (size_t)ee * 4);
    }
    for (int i = tid; i < Bk / 2; i += K1T) hist[i] = 0;
    asm volatile("s_waitcnt lgkmcnt(0)" ::: "memory");
    __builtin_amdgcn_s_barrier();
    u32 br[EPT];
#pragma unroll
    for (int i = 0; i < EPT; ++i) {
      const int e = e0 + tid + i * K1T;
      const u32 bk = divm((u32)(dv[i] + 1) * (u32)Bk - 1u, magN);
      const u32 sh = (bk & 1u) << 4;
      u32 r = 0;
      if (e < e1) r = atomicAdd(&hist[bk >> 1], 1u << sh);
      br[i] = bk | ((r >> sh) << 16);
    }
    asm volatile("s_waitcnt lgkmcnt(0)" ::: "memory");
    __builtin_amdgcn_s_barrier();
    u32 carry = 0;
    for (int sb0 = 0; sb0 < Bk / 4; sb0 += K1T) {
      const int sbi = sb0 + tid;
      u32x2 cn = {0u, 0u};
      if (sbi < Bk / 4) cn = *(const u32x2*)(hist + sbi * 2);
      const u32 k0 = cn[0] & 0xffffu, k1 = cn[0] >> 16, k2 = cn[1] & 0xffffu, k3 = cn[1] >> 16;
      const u32 loc = k0 + k1 + k2 + k3;
      const u32 inc = wave_scan_add(loc);
      if (lane == 63) wsum[w] = inc;
      asm volatile("s_waitcnt lgkmcnt(0)" ::: "memory");
      __builtin_amdgcn_s_barrier();
      const u32x4 s0 = *(const u32x4*)wsum, s1 = *(const u32x4*)(wsum + 4);
      const u32 base = carry + inc - loc + (w > 0 ? s0[0] : 0u) + (w > 1 ? s0[1] : 0u) + (w > 2 ? s0[2] : 0u) + (w > 3 ? s0[3] : 0u) +
                       (w > 4 ? s1[0] : 0u) + (w > 5 ? s1[1] : 0u) + (w > 6 ? s1[2] : 0u);
      carry += s0[0] + s0[1] + s0[2] + s0[3] + s1[0] + s1[1] + s1[2] + s1[3];
      if (sbi < Bk / 4) {
        const u32 b1 = base + k0, b2 = b1 + k1, b3 = b2 + k2;
        *(u32x2*)(hist + sbi * 2) = u32x2{base | (b1 << 16), b2 | (b3 << 16)};
        *(u32x4*)(off + ((size_t)sbi * NCH + (c & 7) * (NCH / 8) + (c >> 3)) * 4) = u32x4{base | (k0 << 16), b1 | (k1 << 16), b2 | (k2 << 16), b3 | (k3 << 16)};
      }
      asm volatile("s_waitcnt lgkmcnt(0)" ::: "memory");
      __builtin_amdgcn_s_barrier();
    }
#pragma unroll
    for (int i = 0; i < EPT; ++i) {
      const int e = e0 + tid + i * K1T;
      if (e < e1) {
        const u32 bk = br[i] & 0xffffu;
        const u32 pos = (u32)((const u16*)hist)[bk] + (br[i] >> 16);
        const u32 dl = (u32)dv[i] - divm(bk * (u32)N, magB);
        u32* d = stage + pos * 3;
        d[0] = ev[i][0]; d[1] = ev[i][1]; d[2] = (u32)sv[i] | (dl << 24);
      }
    }
    asm volatile("s_waitcnt lgkmcnt(0)" ::: "memory");
    __builtin_amdgcn_s_barrier();
    {
      const int nq = ((e1 - e0) * 3 + 3) >> 2;
      u32x4* g = (u32x4*)(rec + (size_t)c * CH * 3);
      for (int i = tid; i < nq; i += K1T) g[i] = *(const u32x4*)(stage + i * 4);
    }
    return;
  }
  const int g = blockIdx.x - NCH;
  const int mat = (g >> 3) & 3, gi = (g & 7) + 8 * (g >> 5);
  const int r0 = gi * 128;
  const int q = lane >> 4, qq = (lane >> 2) & 3, p = lane & 3, nl = lane & 15;
  const u32 XO = 32768, MO = 65536;
  if (g == 0) {
    const int s = tid >> 8, j = (tid >> 6) & 3, cb = (tid >> 4) & 3, n = tid & 15;
    const int lam = 16 * (n >> 2) + 4 * cb + (n & 3);
    const float v = __uint_as_float((u32)wc[j * 128 + 64 * s + lam] << 16) * NLOG2E;
    wcp[tid] = (u16)(pk_bf16(v, 0.f) & 0xffffu);
  }
  if (r0 >= N) { if (mat == 1 && tid == 0) vmax[gi] = 0u; return; }
  {
    const u16* W = mat == 0 ? wb : mat == 1 ? wv : mat == 2 ? wa : wu;
#pragma unroll
    for (int i = 0; i < 4; ++i) {
      const int m = w * 4 + i;
      const int k = m * 4 + q;
      dma_g(lb + XO + m * 1024, x, (u32)(min(r0 + k, N - 1) * 256 + ((nl ^ (k & 15)) << 4)));
    }
#pragma unroll
    for (int i = 0; i < 4; ++i) {
      const int m = w * 4 + i;
      const int k = m * 4 + q;
      const int fw = (k & 1) | ((k & 2) << 2);
      dma_g(lb + m * 1024, W, (u32)(k * 256 + ((nl ^ fw) << 4)));
    }
  }
  if (tid == 0) lds_st<u32>(L + MO, 0u);
  asm volatile("s_waitcnt vmcnt(0) lgkmcnt(0)" ::: "memory");
  __builtin_amdgcn_s_barrier();
  const int s = (w >> 1) & 1, kq = w & 1, hb = w >> 2;
  s16x8 wf[2][4];
#pragma unroll
  for (int be = 0; be < 2; ++be) {
    const int lamB = 16 * p + 8 * kq + 4 * be, lamU = 32 * kq + 8 * p + 4 * be, lamV = 16 * (2 * (p & 1) + be) + 4 * (2 * kq + (p >> 1));
    const int lam0 = mat == 0 ? lamB : mat == 3 ? lamU : lamV;
    const int byte = 2 * (64 * s + lam0);
#pragma unroll
    for (int ks = 0; ks < 4; ++ks) {
      s16x4 h[2];
#pragma unroll
      for (int hh = 0; hh < 2; ++hh) {
        const int k = 32 * ks + 8 * q + 4 * hh + qq;
        const int fw = (k & 1) | ((k & 2) << 2);
        h[hh] = lds_tr(L + (k * 256 + (((byte >> 4) ^ fw) << 4) + (byte & 8)));
      }
      wf[be][ks] = __builtin_shufflevector(h[0], h[1], 0, 1, 2, 3, 4, 5, 6, 7);
    }
  }
  u32 vm = 0;
  char* dbase = ((mat & 2) ? AU : T) + (size_t)s * N * 256 + (mat & 1) * 128 + (4 * kq + q) * 16;
  const float scl = (mat & 1) ? 1.f : NLOG2E;
#pragma unroll
  for (int bi = 0; bi < 4; ++bi) {
    const int lr = (hb * 4 + bi) * 16 + nl;
    f32x4 a0 = {0.f, 0.f, 0.f, 0.f}, a1 = {0.f, 0.f, 0.f, 0.f};
#pragma unroll
    for (int ks = 0; ks < 4; ++ks) {
      const s16x8 xf = lds_ld<s16x8>(L + (XO + lr * 256 + (((4 * ks + q) ^ nl) << 4)));
      a0 = __builtin_amdgcn_mfma_f32_16x16x32_bf16(wf[0][ks], xf, a0, 0, 0, 0);
      a1 = __builtin_amdgcn_mfma_f32_16x16x32_bf16(wf[1][ks], xf, a1, 0, 0, 0);
    }
    u32x4 o;
    if (mat == 1) {
      h16x2 h0 = {(_Float16)a0[0], (_Float16)a0[1]}, h1 = {(_Float16)a0[2], (_Float16)a0[3]};
      h16x2 h2 = {(_Float16)a1[0], (_Float16)a1[1]}, h3 = {(_Float16)a1[2], (_Float16)a1[3]};
      o[0] = __builtin_bit_cast(u32, h0); o[1] = __builtin_bit_cast(u32, h1);
      o[2] = __builtin_bit_cast(u32, h2); o[3] = __builtin_bit_cast(u32, h3);
#pragma unroll
      for (int i = 0; i < 4; ++i) { vm = umax(vm, __float_as_uint(a0[i]) & 0x7fffffffu); vm = umax(vm, __float_as_uint(a1[i]) & 0x7fffffffu); }
    } else {
      o[0] = pk_bf16(a0[0] * scl, a0[1] * scl); o[1] = pk_bf16(a0[2] * scl, a0[3] * scl);
      o[2] = pk_bf16(a1[0] * scl, a1[1] * scl); o[3] = pk_bf16(a1[2] * scl, a1[3] * scl);
    }
    if (r0 + lr < N) *(u32x4*)(dbase + (size_t)(r0 + lr) * 256) = o;
  }
  if (mat == 1) {
    const u32 m = wave_scan_max(vm);
    if (lane == 63) atomicMax((u32*)(smem + MO), m);
    asm volatile("s_waitcnt lgkmcnt(0)" ::: "memory");
    __builtin_amdgcn_s_barrier();
    if (tid == 0) vmax[gi] = lds_ld<u32>(L + MO);
  }
}

constexpr u32 STG = 8192;
constexpr u32 OHR = 4096, IDR = 512;
constexpr u32 K2LDS = 4 * STG + 4 * (OHR + IDR) + 1024;

__global__ __launch_bounds__(256, 3) void k_edge(const u16* __restrict__ x, const u16* __restrict__ wv, const u16* __restrict__ wcp, const char* __restrict__ T,
                                                const char* __restrict__ AU, const u32* __restrict__ rec,
                                                const u32* __restrict__ off, const u32* __restrict__ vmax,
                                                u16* __restrict__ out, u64 magB, int N, int Bk, int CH, int nvm) {
  extern __shared__ __attribute__((aligned(16))) char smem[];
  const lptr L = (lptr)smem;
  const u32 lb = (u32)(uintptr_t)L;
  const int tid = threadIdx.x, lane = tid & 63;
  const int w = __builtin_amdgcn_readfirstlane(tid >> 6);
  const int r8 = blockIdx.x & 7, slice = r8 & 1;
  const int sb = (blockIdx.x >> 3) * 4 + (r8 >> 1);
  const int b = sb * 4 + w;
  const int q = lane >> 4, nl = lane & 15, qq = (lane >> 2) & 3, p = lane & 3;
  const lptr stg = L + w * STG, ohr = L + 4 * STG + w * (OHR + IDR), idr = ohr + OHR;
  const u32 stgm0 = lb + w * STG;

  const int n0 = (int)divm((u32)b * (u32)N, magB), n1 = (int)divm((u32)(b + 1) * (u32)N, magB);
  const int nb = n1 - n0;
  const char* Ts = T + (size_t)slice * N * 256;
  const char* AUs = AU + (size_t)slice * N * 256;
  const u32x4 o4 = *(const u32x4*)(off + ((size_t)sb * NCH + lane) * 4);
  const u32 cid = (u32)((lane % (NCH / 8)) * 8 + lane / (NCH / 8));
  {
    const int rr = lane >> 3, pos = lane & 7;
    const int r0 = min(n0 + min(rr, max(nb - 1, 0)), N - 1), r1 = min(n0 + min(8 + rr, max(nb - 1, 0)), N - 1);
    const u32 awm0 = lb + 4 * STG + w * (OHR + IDR) + 2048;
    dma_g(awm0, AUs, (u32)(r0 * 256 + pos * 16));
    const char* s1 = rr < 4 ? AUs + (size_t)(r1 * 256 + pos * 16) : (const char*)wcp + (slice * 512 + (rr - 4) * 128 + pos * 16);
    asm volatile("s_mov_b32 m0, %0\n\ts_nop 0\n\tglobal_load_lds_dwordx4 %1, off" ::"s"(awm0 + 1024), "v"(s1) : "memory");
  }
  u32 vmx = 0;
  for (int i = lane; i < nvm; i += 64) vmx = umax(vmx, vmax[i]);

  const u32 swz = (u32)(nl & 7) << 1;
  lptr aB0 = stg + (nl * 256 + ((q ^ swz) << 4)), aB1 = stg + (nl * 256 + (((4 + q) ^ swz) << 4));
  lptr aV[4];
  {
    const u32 row = 4 * q + qq, sw = (row & 7) << 1;
#pragma unroll
    for (int cb = 0; cb < 4; ++cb) aV[cb] = stg + (row * 256 + (((8 + 2 * cb + (p >> 1)) ^ sw) << 4) + (p & 1) * 8);
  }
  lptr aOH = ohr + (nl * 32 + q * 8), aS = ohr + lane * 8, aI = idr + q * 2;
  u32 voff[4];
  voff[0] = voff[2] = ((u32)nl ^ ((u32)q << 1)) << 4; voff[1] = voff[3] = ((u32)nl ^ ((u32)(4 + q) << 1)) << 4;
  u32x4 rsrc;
  {
    const u64 tbp = (u64)(uintptr_t)Ts;
    rsrc[0] = (u32)tbp; rsrc[1] = ((u32)(tbp >> 32) & 0xffffu) | (256u << 16); rsrc[2] = (u32)N; rsrc[3] = 0x00020000u;
  }
  s16x4 ident;
#pragma unroll
  for (int j = 0; j < 4; ++j) ident[j] = (4 * q + j == nl) ? (short)0x3f80 : (short)0;
  f32x4 Yd[4], Yn[4];
#pragma unroll
  for (int cb = 0; cb < 4; ++cb) { Yd[cb] = f32x4{0.f, 0.f, 0.f, 0.f}; Yn[cb] = f32x4{0.f, 0.f, 0.f, 0.f}; }
  asm volatile("" : "+v"(aB0), "+v"(aB1), "+v"(aV[0]), "+v"(aV[1]), "+v"(aV[2]), "+v"(aV[3]), "+v"(aOH), "+v"(aS), "+v"(aI));
  asm volatile("" : "+v"(voff[0]), "+v"(voff[1]), "+v"(voff[2]), "+v"(voff[3]), "+v"(ident));

  const u32 gst = cid * (u32)CH + (o4[0] & 0xffffu);
  const u32 rlen = (o4[3] & 0xffffu) + (o4[3] >> 16) - (o4[0] & 0xffffu);
  {
    const u32 l31 = (u32)lane & 31u;
#pragma unroll
    for (int m = 0; m < 8; ++m) {
      const u32 ga = __builtin_amdgcn_readlane(gst, w * 16 + 2 * m), gb = __builtin_amdgcn_readlane(gst, w * 16 + 2 * m + 1);
      const u32 la = __builtin_amdgcn_readlane(rlen, w * 16 + 2 * m), lb2 = __builtin_amdgcn_readlane(rlen, w * 16 + 2 * m + 1);
      const u32 vo = ((lane < 32 ? ga : gb) + l31) * 12u;
      if (l31 < (lane < 32 ? la : lb2))
        asm volatile("s_mov_b32 m0, %0\n\ts_nop 0\n\tglobal_load_lds_dwordx3 %1, %2" ::"s"(lb + (w * 16 + 2 * m) * 512), "v"(vo), "s"(rec) : "memory");
    }
  }
  vmx = __builtin_amdgcn_readlane(wave_scan_max(vmx), 63);
  if (vmx != 0u && (vmx < 0x38800000u || vmx > 0x43000000u)) {
    asm volatile("s_waitcnt vmcnt(0) lgkmcnt(0)" ::: "memory");
    __builtin_amdgcn_s_barrier();
    const int lf = fresh_lane();
    const u32 ow = off[((size_t)sb * NCH + lf) * 4 + w];
    const lptr acc = stg + lf * 4;
    for (int n = 0; n < 24; ++n) lds_st<float>(acc + n * 256, 0.f);
    const int posB = ((lf >> 4) + 4 * ((lf >> 3) & 1)) * 16 + (lf & 7) * 2;
    const int pi = 16 * ((lf >> 2) & 3) + 4 * (lf >> 4) + (lf & 3);
    float wcf[4];
#pragma unroll
    for (int j = 0; j < 4; ++j) wcf[j] = bf_lo(wcp[slice * 256 + j * 64 + pi]);
    for (int t = 0; t < NCH; ++t) {
      const u32 ot = __builtin_amdgcn_readlane(ow, t);
      const u32 base = (u32)((t % (NCH / 8)) * 8 + t / (NCH / 8)) * (u32)CH + (ot & 0xffffu);
      for (u32 r = 0; r < (ot >> 16); ++r) {
        const u32* rp = rec + (size_t)(base + r) * 3;
        const u32 e01 = rp[0], e23 = rp[1], wd = rp[2];
        const u32 sn = wd & 0xffffffu, dl = (wd >> 24) & 15u;
        float tv = bf_lo(*(const u16*)(AUs + (size_t)(n0 + dl) * 256 + 2 * pi)) + bf_lo(*(const u16*)(Ts + (size_t)sn * 256 + posB));
        tv += bf_lo(e01) * wcf[0] + bf_hi(e01) * wcf[1] + bf_lo(e23) * wcf[2] + bf_hi(e23) * wcf[3];
        const float eta = 1.f / (1.f + __builtin_amdgcn_exp2f(tv));
        float v = 0.f;
        for (int k = 0; k < 128; ++k) v += bf_lo(x[(size_t)sn * 128 + k]) * bf_lo(wv[k * 128 + slice * 64 + lf]);
        lds_st<float>(acc + dl * 512, lds_ld<float>(acc + dl * 512) + eta);
        lds_st<float>(acc + dl * 512 + 256, lds_ld<float>(acc + dl * 512 + 256) + eta * v);
      }
    }
    for (int n = 0; n < nb; ++n) {
      const size_t o = (size_t)(n0 + n) * 128 + slice * 64 + lf;
      const float z = bf_lo(*(const u16*)(AUs + (size_t)(n0 + n) * 256 + 128 + lf * 2)) +
                      lds_ld<float>(acc + n * 512 + 256) / (lds_ld<float>(acc + n * 512) + 1e-6f);
      out[o] = (u16)(pk_bf16(bf_lo(x[o]) + (z > 0.f ? z : 0.f * z), 0.f) & 0xffffu);
    }
    return;
  }
  int ea_ = 15 - ((int)(vmx >> 23) - 126);
  ea_ = ea_ > 15 ? 15 : ea_;
  const float cneg = (float)(-ea_), cadd = __uint_as_float((u32)(127 - ea_) << 23);
  const float epsv = 1e-6f * 1.875f * __uint_as_float((u32)(127 + ea_) << 23);
  const f32x2 cadd2 = {cadd, cadd};
  const f32x4 cinit = {0.f, 0.f, 0.f, 0.f};
  const u32 o0 = w == 0 ? o4[0] : w == 1 ? o4[1] : w == 2 ? o4[2] : o4[3];
  u32x3 raw[NG];
  const u32x3 padrec = {0u, 0u, 15u << 24};
  const u32 inc0 = wave_scan_add(o0 >> 16);
  const int ntot = (int)__builtin_amdgcn_readlane(inc0, 63);
  const bool fast = ntot <= WIN;
  const u32 gd0 = cid * (u32)CH + (o0 & 0xffffu) - (inc0 - (o0 >> 16));
  auto setup = [&](int w0) {
    const int ln = fresh_lane();
    const int nw = min(ntot - w0, WIN);
    const u32 oo = off[((size_t)sb * NCH + ln) * 4 + w];
    const u32 c0 = oo >> 16;
    const u32 pre0 = wave_scan_add(c0) - c0;
    const u32 gd = (u32)((ln % (NCH / 8)) * 8 + ln / (NCH / 8)) * (u32)CH + (oo & 0xffffu) - pre0;
    const lptr mk = ohr, dt = ohr + 512;
    lds_st<u32x2>(mk + ln * 8, u32x2{0u, 0u});
    lds_st<u32>(dt + ln * 4, gd);
    __builtin_amdgcn_wave_barrier();
    if (c0 && (int)(pre0 + c0) > w0 && (int)pre0 < w0 + WIN) lds_st<unsigned char>(mk + max((int)pre0 - w0, 0), (unsigned char)(ln + 1));
    __builtin_amdgcn_wave_barrier();
    u32 carry = 0;
#pragma unroll
    for (int g = 0; g < NG; ++g) {
      raw[g] = padrec;
      if (g * 64 < nw) {
        u32 id = lds_ld<unsigned char>(mk + ln + g * 64);
        id = umax(wave_scan_max(id), carry);
        carry = __builtin_amdgcn_readlane(id, 63);
        const int j = g * 64 + ln;
        const u32 d = lds_ld<u32>(dt + (id - 1) * 4);
        if (j < nw) raw[g] = *(const u32x3*)(rec + (size_t)(u32)(w0 + j + d) * 3);
      }
    }
    __builtin_amdgcn_wave_barrier();
  };
  u32x2 en[NG];
  if (fast) {
    const u32 c0 = o0 >> 16, pre0 = inc0 - c0;
    const u32 ra = (o0 & 0xffffu) - (o4[0] & 0xffffu);
    const lptr mk = ohr, dt = ohr + 512;
    lds_st<u32x2>(mk + lane * 8, u32x2{0u, 0u});
    const int lim = max(min((int)pre0 + 32 - (int)ra, 0x7fff), 0);
    lds_st<u32x2>(dt + lane * 8, u32x2{(((u32)lane * 32u + ra - pre0) & 0xffffu) | ((u32)lim << 16), gd0});
    __builtin_amdgcn_wave_barrier();
    if (c0) lds_st<unsigned char>(mk + pre0, (unsigned char)(lane + 1));
    __builtin_amdgcn_wave_barrier();
    u32 carry = 0;
#pragma unroll
    for (int g = 0; g < NG; ++g) {
      if (g * 64 < ntot) {
        u32 id = lds_ld<unsigned char>(mk + lane + g * 64);
        id = umax(wave_scan_max(id), carry);
        carry = __builtin_amdgcn_readlane(id, 63);
        en[g] = lds_ld<u32x2>(dt + (id - 1) * 8);
      }
    }
  }
  asm volatile("s_waitcnt vmcnt(0) lgkmcnt(0)" ::: "memory");
  __builtin_amdgcn_s_barrier();
  s16x4 AW[4];
#pragma unroll
  for (int cb = 0; cb < 4; ++cb) {
    AW[cb] = lds_tr(ohr + (2048 + (4 * q + qq) * 128 + cb * 32 + p * 8));
    if (q == 2) AW[cb][3] = (short)(__float_as_uint(cneg) >> 16);
  }
  if (fast) {
#pragma unroll
    for (int g = 0; g < NG; ++g) {
      raw[g] = padrec;
      if (g * 64 < ntot) {
        const int j = g * 64 + lane;
        const bool ovf = j >= (int)(en[g][0] >> 16);
        if (j < ntot) {
          if (!ovf) raw[g] = lds_ld<u32x3>(L + ((j + (int)(short)en[g][0]) << 4));
          else raw[g] = *(const u32x3*)(rec + (size_t)(u32)(j + en[g][1]) * 3);
        }
      }
    }
  }
  asm volatile("s_waitcnt lgkmcnt(0)" ::: "memory");
  __builtin_amdgcn_s_barrier();
  auto expand = [&](const u32x3& r, int half) {
    const lptr a = ohr + lane * 32 + half * 2048;
    lds_st<u32x4>(a, u32x4{0u, 0u, 0u, 0u});
    lds_st<u32x4>(a + 16, u32x4{0u, 0x3f800000u, r[0], r[1]});
    const u32 dl = (r[2] >> 24) & 15u;
    if (dl < 11) lds_st<u16>(a + dl * 2, (u16)0x3f80);
    lds_st<u16>(idr + lane * 2 + half * 128, (u16)r[2]);
  };
#pragma nounroll
  for (int w0 = 0; w0 < ntot; w0 += WIN) {
    if (w0 || !fast) setup(w0);
    const int nw = min(ntot - w0, WIN);
    asm volatile("" : "+v"(raw[0]), "+v"(raw[1]), "+v"(raw[2]), "+v"(raw[3]), "+v"(raw[4]), "+v"(raw[5]));
    expand(raw[0], 0);
    expand(raw[1], 1);
    const int nt = (nw + 15) >> 4;
    u32 id[4];
    auto idx_read = [&](int rt) {
#pragma unroll
      for (int k = 0; k < 4; ++k) id[k] = lds_ld<u16>(aI + rt * 32 + k * 8);
    };
    auto dma_go = [&](int slot) {
      asm volatile("s_waitcnt lgkmcnt(0)" ::: "memory");
#pragma unroll
      for (int k = 0; k < 4; ++k) dma_b(stgm0 + slot * 4096 + k * 1024, rsrc, u32x2{id[k], voff[k]});
    };
    s16x8 b0, b1;
    s16x4 oh, vv[4];
    h16x4 sm;
    idx_read(0); dma_go(0);
    idx_read(1); dma_go(1);
    asm volatile("s_waitcnt vmcnt(4)" ::: "memory");
    b0 = lds_ld<s16x8>(aB0); b1 = lds_ld<s16x8>(aB1); oh = lds_ld<s16x4>(aOH);
#pragma unroll
    for (int cb = 0; cb < 4; ++cb) vv[cb] = lds_tr(aV[cb]);
    sm = __builtin_bit_cast(h16x4, lds_tr(aS));
    idx_read(2); dma_go(0);
#define GATE(cb)                                                                                                   \
      {                                                                                                            \
        f32x2 d0 = {__builtin_amdgcn_exp2f(t[cb][0]), __builtin_amdgcn_exp2f(t[cb][1])};                           \
        f32x2 d1 = {__builtin_amdgcn_exp2f(t[cb][2]), __builtin_amdgcn_exp2f(t[cb][3])};                           \
        d0 += cadd2; d1 += cadd2;                                                                                  \
        const h16x2 e0 = __builtin_bit_cast(h16x2, __builtin_amdgcn_cvt_pkrtz(__builtin_amdgcn_rcpf(d0[0]), __builtin_amdgcn_rcpf(d0[1]))); \
        const h16x2 e1 = __builtin_bit_cast(h16x2, __builtin_amdgcn_cvt_pkrtz(__builtin_amdgcn_rcpf(d1[0]), __builtin_amdgcn_rcpf(d1[1]))); \
        const h16x4 e = __builtin_shufflevector(e0, e1, 0, 1, 2, 3);                                               \
        const h16x4 pr = e * __builtin_bit_cast(h16x4, cv[cb]);                                                    \
        Yd[cb] = __builtin_amdgcn_mfma_f32_16x16x16f16(e, cs, Yd[cb], 0, 0, 0);                                    \
        Yn[cb] = __builtin_amdgcn_mfma_f32_16x16x16f16(pr, cs, Yn[cb], 0, 0, 0);                                   \
      }
#define TILE_STEP(RT)                                                                                              \
    {                                                                                                              \
      constexpr int SN = ((RT) + 1) & 1, RN = ((RT) + 1) & 7;                                                      \
      f32x4 t[4];                                                                                                  \
      _Pragma("unroll") for (int cb = 0; cb < 4; ++cb)                                                             \
        t[cb] = __builtin_amdgcn_mfma_f32_16x16x16bf16_1k(oh, AW[cb], cinit, 0, 0, 0);                             \
      _Pragma("unroll") for (int cb = 0; cb < 4; ++cb) {                                                           \
        const s16x8 bb = (cb & 2) ? b1 : b0;                                                                       \
        const s16x4 bf = (cb & 1) ? __builtin_shufflevector(bb, bb, 4, 5, 6, 7) : __builtin_shufflevector(bb, bb, 0, 1, 2, 3); \
        t[cb] = __builtin_amdgcn_mfma_f32_16x16x16bf16_1k(bf, ident, t[cb], 0, 0, 0);                              \
      }                                                                                                            \
      const s16x4 cv[4] = {vv[0], vv[1], vv[2], vv[3]};                                                            \
      const h16x4 cs = sm;                                                                                         \
      GATE(0)                                                                                                      \
      asm volatile("s_waitcnt vmcnt(4)" ::: "memory");                                                             \
      b0 = lds_ld<s16x8>(aB0 + SN * 4096); b1 = lds_ld<s16x8>(aB1 + SN * 4096); oh = lds_ld<s16x4>(aOH + RN * 512); \
      _Pragma("unroll") for (int cb = 0; cb < 4; ++cb) vv[cb] = lds_tr(aV[cb] + SN * 4096);                        \
      sm = __builtin_bit_cast(h16x4, lds_tr(aS + RN * 512));                                                       \
      idx_read(((RT) + 3) & 7);                                                                                    \
      GATE(1) GATE(2)                                                                                              \
      dma_go(SN);                                                                                                  \
      GATE(3)                                                                                                      \
    }
#pragma nounroll
    for (int i0 = 0; i0 < nt; i0 += 8) {
      TILE_STEP(0)
      if (i0 + 1 < nt) {
        TILE_STEP(1)
        if (i0 + 2 < nt) {
          TILE_STEP(2)
          if (i0 + 3 < nt) {
            TILE_STEP(3)
            if (i0 == 0) {
              expand(raw[2], 0);
              const int lp = fresh_lane();
              const int er = min(n0 + min(lp & 15, max(nb - 1, 0)), N - 1);
              const char* s2 = (lp & 16) ? AUs + (size_t)(er * 256 + 128) : (const char*)x + ((size_t)er * 256 + slice * 128);
              asm volatile("s_mov_b32 m0, %0\n\ts_nop 0\n\tglobal_load_lds_dword %1, off" ::"s"(lb + 4 * STG + w * (OHR + IDR) + OHR + 256), "v"(s2) : "memory");
            } else if (i0 == 8) expand(raw[4], 0);
            if (i0 + 4 < nt) {
              TILE_STEP(4)
              if (i0 + 5 < nt) {
                TILE_STEP(5)
                if (i0 + 6 < nt) {
                  TILE_STEP(6)
                  if (i0 + 7 < nt) {
                    TILE_STEP(7)
                    if (i0 == 0) expand(raw[3], 1); else if (i0 == 8) expand(raw[5], 1);
                  }
                }
              }
            }
          }
        }
      }
    }
    asm volatile("s_waitcnt vmcnt(0) lgkmcnt(0)" ::: "memory");
#undef TILE_STEP
#undef GATE
  }
  const int le = fresh_lane(), ne = le & 15, qe = le >> 4;
  if (ne < nb) {
    const size_t o = (size_t)(n0 + ne) * 128 + slice * 64 + qe * 16;
    const u32x4 x0 = *(const u32x4*)(x + o), x1 = *(const u32x4*)(x + o + 8);
    const char* up = AUs + (size_t)(n0 + ne) * 256 + 128 + qe * 32;
    const u32x4 u0 = *(const u32x4*)up, u1 = *(const u32x4*)(up + 16);
    u32x4 r0, r1;
#pragma unroll
    for (int cb = 0; cb < 4; ++cb) {
      const int ix = (cb & 1) * 2;
      const u32 ua = (cb & 2) ? u1[ix] : u0[ix], ub = (cb & 2) ? u1[ix + 1] : u0[ix + 1];
      const float uv[4] = {bf_lo(ua), bf_hi(ua), bf_lo(ub), bf_hi(ub)};
      float z[4];
#pragma unroll
      for (int i = 0; i < 4; ++i) {
        const float agg = Yn[cb][i] * __builtin_amdgcn_rcpf(Yd[cb][i] + epsv);
        const float zz = uv[i] + agg;
        z[i] = zz > 0.f ? zz : 0.f * zz;
      }
      const u32 xa = (cb & 2) ? x1[ix] : x0[ix], xb = (cb & 2) ? x1[ix + 1] : x0[ix + 1];
      const u32 ra = pk_bf16(bf_lo(xa) + z[0], bf_hi(xa) + z[1]), rb = pk_bf16(bf_lo(xb) + z[2], bf_hi(xb) + z[3]);
      if (cb & 2) { r1[ix] = ra; r1[ix + 1] = rb; } else { r0[ix] = ra; r0[ix + 1] = rb; }
    }
    *(u32x4*)(out + o) = r0;
    *(u32x4*)(out + o + 8) = r1;
  }
}

__global__ void g_gemm(const u16* __restrict__ x, const u16* __restrict__ wa, const u16* __restrict__ wb, const u16* __restrict__ wu,
                       const u16* __restrict__ wv, float* __restrict__ P, size_t sz) {
  const size_t n = blockIdx.x;
  const int d = threadIdx.x;
  float a = 0.f, b = 0.f, u = 0.f, v = 0.f;
  for (int k = 0; k < 128; ++k) {
    const float xv = bf_lo(x[n * 128 + k]);
    a += xv * bf_lo(wa[k * 128 + d]); b += xv * bf_lo(wb[k * 128 + d]);
    u += xv * bf_lo(wu[k * 128 + d]); v += xv * bf_lo(wv[k * 128 + d]);
  }
  const size_t i = n * 128 + d;
  P[i] = a; P[sz + i] = b; P[2 * sz + i] = u; P[3 * sz + i] = v; P[4 * sz + i] = 0.f; P[5 * sz + i] = 0.f;
}
__global__ void g_edge(const u16* __restrict__ ea, const int* __restrict__ src, const int* __restrict__ dst, const u16* __restrict__ wc,
                       float* __restrict__ P, size_t sz, int E) {
  const int e = blockIdx.x * 2 + (threadIdx.x >> 7), d = threadIdx.x & 127;
  if (e >= E) return;
  const size_t s = (size_t)src[e] * 128 + d, t = (size_t)dst[e] * 128 + d;
  float g = P[t] + P[sz + s];
  for (int k = 0; k < 4; ++k) g += bf_lo(ea[(size_t)e * 4 + k]) * bf_lo(wc[k * 128 + d]);
  const float eta = 1.f / (1.f + __builtin_amdgcn_exp2f(g * NLOG2E));
  atomicAdd(&P[4 * sz + t], eta);
  atomicAdd(&P[5 * sz + t], eta * P[3 * sz + s]);
}
__global__ void g_fin(const u16* __restrict__ x, const float* __restrict__ P, u16* __restrict__ out, size_t sz) {
  const size_t i = (size_t)blockIdx.x * 256 + threadIdx.x;
  if (i >= sz) return;
  const float z = P[2 * sz + i] + P[5 * sz + i] / (P[4 * sz + i] + 1e-6f);
  out[i] = (u16)(pk_bf16(bf_lo(x[i]) + (z > 0.f ? z : 0.f * z), 0.f) & 0xffffu);
}

extern "C" void kernel_launch(void* const* d_in, const int* in_sizes, int n_in, void* d_out, int out_size, void* d_ws,
                              size_t ws_size, hipStream_t stream) {
  const int N = in_sizes[0] / 128, E = in_sizes[2];
  int Bk = 1536;
  if (N > 10 * Bk) Bk = ((N + 9) / 10 + 15) / 16 * 16;
  const int CH = ((E + NCH - 1) / NCH + 3) & ~3;
  const int GW = ((N + 127) / 128 + 7) / 8 * 8;
  if (N <= 0) return;
  if (N > 65535 || E <= 0 || CH > EPT * K1T) {
    const size_t sz = (size_t)N * 128;
    float* P = (float*)d_ws;
    g_gemm<<<N, 128, 0, stream>>>((const u16*)d_in[0], (const u16*)d_in[4], (const u16*)d_in[5], (const u16*)d_in[7], (const u16*)d_in[8], P, sz);
    if (E > 0) g_edge<<<(E + 1) / 2, 256, 0, stream>>>((const u16*)d_in[1], (const int*)d_in[2], (const int*)d_in[3], (const u16*)d_in[6], P, sz, E);
    g_fin<<<(int)((sz + 255) / 256), 256, 0, stream>>>((const u16*)d_in[0], P, (u16*)d_out, sz);
    return;
  }
  char* ws = (char*)d_ws;
  size_t o = 0;
  char* T = ws + o; o += (size_t)2 * N * 256; o = (o + 255) & ~(size_t)255;
  char* AU = ws + o; o += (size_t)2 * N * 256; o = (o + 255) & ~(size_t)255;
  u32* rec = (u32*)(ws + o); o += (size_t)NCH * CH * 12 + 512; o = (o + 255) & ~(size_t)255;
  u32* off = (u32*)(ws + o); o += (size_t)NCH * Bk * 4; o = (o + 255) & ~(size_t)255;
  u32* vmax = (u32*)(ws + o); o += (size_t)GW * 8 * 4; o = (o + 255) & ~(size_t)255;
  u16* wcp = (u16*)(ws + o);
  const u64 magN = ((1ull << 48) + (u64)N - 1) / (u64)N, magB = ((1ull << 48) + (u64)Bk - 1) / (u64)Bk;
  int lds1 = Bk * 2 + 64 + CH * 12 + 16;
  if (lds1 < 65536 + 16) lds1 = 65536 + 16;
  (void)hipFuncSetAttribute((const void*)k_prep, hipFuncAttributeMaxDynamicSharedMemorySize, lds1);
  k_prep<<<NCH + 4 * GW, K1T, lds1, stream>>>((const u16*)d_in[0], (const u16*)d_in[1], (const int*)d_in[2], (const int*)d_in[3],
                                             (const u16*)d_in[4], (const u16*)d_in[5], (const u16*)d_in[6], (const u16*)d_in[7],
                                             (const u16*)d_in[8], T, AU, rec, off, vmax, wcp, magN, magB, N, E, Bk, CH, GW);
  k_edge<<<Bk / 4 * 2, 256, K2LDS, stream>>>((const u16*)d_in[0], (const u16*)d_in[8], wcp, T, AU, rec, off, vmax, (u16*)d_out, magB, N, Bk, CH, GW);
}
```
